```python
import torch

def _softmax(x, axis=-1):
    return torch.softmax(x, dim=axis)
def _gelu(x):
    return torch.nn.functional.gelu(x, approximate="tanh")
def _silu(x):
    return torch.nn.functional.silu(x)
def _sigmoid(x):
    return torch.sigmoid(x)
def _layernorm(x, axis=-1, eps=1e-5):
    m = x.mean(dim=axis, keepdim=True)
    v = x.var(dim=axis, keepdim=True, unbiased=False)
    return (x - m) / torch.sqrt(v + eps)
def _scatter_add(tgt, idx, src, axis=0):
    return tgt.clone().index_add_(axis, idx, src)
def _segment_sum(src, seg, n):
    out = torch.zeros((n,) + src.shape[1:], dtype=src.dtype)
    return out.index_add_(0, seg, src)
def _pad(x, pw):
    flat = tuple(v for pair in reversed(pw) for v in pair)
    return torch.nn.functional.pad(x, flat)

kernel_name = 'gather_scatter__hybrid_graph_tf_graphgps__graph'

def setup_inputs():

    _w = (lambda a: (lambda t: t.to(torch.bfloat16) if t.is_floating_point() else t)(torch.as_tensor(a)))
    N, deg, D = 10000, 64, 64
    E = N * deg
    src = (lambda lo, hi, *s: torch.randint(lo, hi, s))(0, N, E).to(torch.int64)
    dst = (lambda lo, hi, *s: torch.randint(lo, hi, s))(0, N, E).to(torch.int64)
    deg_cnt = torch.bincount(dst, minlength=N).to(float) + 1.0
    sc = 1.0 / float(D) ** 0.5
    return {
        "x": _w((lambda *s: torch.randn(*s, dtype=torch.bfloat16) * 0.05)(N, D)),
        "src": _w(src),
        "dst": _w(dst),
        "deg_inv": _w((1.0 / deg_cnt).to(float)),
        "w_msg": _w((lambda *s: torch.randn(*s, dtype=torch.bfloat16) * 0.4)(D, D) * sc),
        "wq": _w((lambda *s: torch.randn(*s, dtype=torch.bfloat16) * 1.6)(D, D) * sc),
        "wk": _w((lambda *s: torch.randn(*s, dtype=torch.bfloat16) * 1.6)(D, D) * sc),
        "wv": _w((lambda *s: torch.randn(*s, dtype=torch.bfloat16) * 0.2)(D, D) * sc),
        "w_out": _w((lambda *s: torch.randn(*s, dtype=torch.bfloat16) * 0.2)(D, D) * sc),
    }

def reference(x, src, dst, deg_inv, w_msg, wq, wk, wv, w_out):
    N, D = x.shape[0], x.shape[1]
    h = _layernorm(x, axis=-1)
    m_e = torch.einsum('nd,de->ne', h, w_msg)
    h_local = _segment_sum(m_e[src], dst, N) * deg_inv[:, None]
    q = torch.einsum('nd,de->ne', h, wq)
    k = torch.einsum('nd,de->ne', h, wk)
    v = torch.einsum('nd,de->ne', h, wv)
    logits = torch.einsum('id,jd->ij', q, k) / (float(D) ** 0.5)
    attn = _softmax(logits, axis=-1)
    h_global = torch.einsum('ij,jd->id', attn, v)
    y = torch.einsum('nd,de->ne', h_local + h_global, w_out)
    return x + y
```

```cpp
#include <hip/hip_runtime.h>
#include <hip/hip_bf16.h>

static __device__ __forceinline__ float bf2f(unsigned short b) { return __uint_as_float(((unsigned)b) << 16); }

__global__ void k_prep(const unsigned short* x, const unsigned short* wm, const unsigned short* wq, const unsigned short* wk,
                       const unsigned short* wv, float* m, float* q, float* k, float* v, float* acc, int N) {
    __shared__ float h[64];
    int n = blockIdx.x, d = threadIdx.x;
    float xv = bf2f(x[n * 64 + d]);
    float s = xv;
    for (int o = 32; o; o >>= 1) s += __shfl_xor(s, o);
    float mean = s / 64.f, c = xv - mean, ss = c * c;
    for (int o = 32; o; o >>= 1) ss += __shfl_xor(ss, o);
    h[d] = c / sqrtf(ss / 64.f + 1e-5f);
    __syncthreads();
    float am = 0, aq = 0, ak = 0, av = 0;
    for (int i = 0; i < 64; ++i) {
        am += h[i] * bf2f(wm[i * 64 + d]); aq += h[i] * bf2f(wq[i * 64 + d]);
        ak += h[i] * bf2f(wk[i * 64 + d]); av += h[i] * bf2f(wv[i * 64 + d]);
    }
    m[n * 64 + d] = am; q[n * 64 + d] = aq; k[n * 64 + d] = ak; v[n * 64 + d] = av; acc[n * 64 + d] = 0.f;
}

__global__ void k_scatter(const int* src, const int* dst, const float* m, float* acc, int E) {
    int e = blockIdx.x * 4 + (threadIdx.x >> 6), d = threadIdx.x & 63;
    if (e < E) atomicAdd(&acc[dst[e] * 64 + d], m[src[e] * 64 + d]);
}

__global__ void k_attn(const unsigned short* x, const unsigned short* dinv, const unsigned short* wo, const float* q, const float* k,
                       const float* v, const float* acc, __hip_bfloat16* out, int N) {
    __shared__ float qs[64], p[64], z[64];
    int n = blockIdx.x, l = threadIdx.x;
    qs[l] = q[n * 64 + l];
    __syncthreads();
    float mx = -INFINITY, sum = 0.f, o = 0.f;
    for (int j0 = 0; j0 < N; j0 += 64) {
        int j = j0 + l;
        float s = -INFINITY;
        if (j < N) { s = 0; for (int i = 0; i < 64; ++i) s += qs[i] * k[j * 64 + i]; s *= 0.125f; }
        float tm = s;
        for (int of = 32; of; of >>= 1) tm = fmaxf(tm, __shfl_xor(tm, of));
        float nm = fmaxf(mx, tm), a = expf(mx - nm), e = expf(s - nm), ts = e;
        for (int of = 32; of; of >>= 1) ts += __shfl_xor(ts, of);
        sum = sum * a + ts; o *= a; mx = nm;
        __syncthreads();
        p[l] = e;
        __syncthreads();
        int cnt = min(64, N - j0);
        for (int i = 0; i < cnt; ++i) o += p[i] * v[(j0 + i) * 64 + l];
    }
    z[l] = o / sum + acc[n * 64 + l] * bf2f(dinv[n]);
    __syncthreads();
    float y = 0;
    for (int i = 0; i < 64; ++i) y += z[i] * bf2f(wo[i * 64 + l]);
    out[n * 64 + l] = __float2bfloat16(bf2f(x[n * 64 + l]) + y);
}

extern "C" void kernel_launch(void* const* d_in, const int* in_sizes, int n_in, void* d_out, int out_size, void* d_ws, size_t ws_size,
                              hipStream_t stream) {
    const unsigned short* x = (const unsigned short*)d_in[0];
    const int* src = (const int*)d_in[1];
    const int* dst = (const int*)d_in[2];
    const unsigned short* dinv = (const unsigned short*)d_in[3];
    int N = in_sizes[0] / 64, E = in_sizes[1];
    float* w = (float*)d_ws;
    float *m = w, *q = w + N * 64, *k = w + 2 * N * 64, *v = w + 3 * N * 64, *acc = w + 4 * N * 64;
    k_prep<<<N, 64, 0, stream>>>(x, (const unsigned short*)d_in[4], (const unsigned short*)d_in[5], (const unsigned short*)d_in[6],
                                 (const unsigned short*)d_in[7], m, q, k, v, acc, N);
    k_scatter<<<(E + 3) / 4, 256, 0, stream>>>(src, dst, m, acc, E);
    k_attn<<<N, 64, 0, stream>>>(x, dinv, (const unsigned short*)d_in[8], q, k, v, acc, (__hip_bfloat16*)d_out, N);
}
```

```cpp
#include <hip/hip_runtime.h>
#include <hip/hip_bf16.h>

typedef unsigned u32x4 __attribute__((ext_vector_type(4)));
typedef unsigned u32x2 __attribute__((ext_vector_type(2)));
typedef int i32x4 __attribute__((ext_vector_type(4)));
typedef float f32x4 __attribute__((ext_vector_type(4)));
typedef float f32x2 __attribute__((ext_vector_type(2)));
typedef __bf16 bf16x8 __attribute__((ext_vector_type(8)));
typedef __bf16 bf16x2 __attribute__((ext_vector_type(2)));
typedef unsigned short u16;
#define DI __device__ __forceinline__
#define LAS __attribute__((address_space(3)))

constexpr int NN = 10000, NE = 640000, NWG = 250, EPC = NE / NWG, NPB = 40;
constexpr unsigned OFF_U = 0, OFF_Q = 1280000, OFF_W = 2560000, OFF_TAB = OFF_W + 16384, OFF_BOX = OFF_TAB + NWG * 1024,
                   BOXB = 2176, OFF_SORT = OFF_BOX + NWG * 3 * BOXB, OFF_DBG = OFF_SORT + NE * 4 + 4096;
constexpr unsigned LROW = 264, SENT = 0xFFFFFFFFu;
constexpr float QSCALE = 0.125f * 1.4426950408889634f;
constexpr float QN2_MAX = 150.0f;

DI float bflo(unsigned w) { return __uint_as_float(w << 16); }
DI float bfhi(unsigned w) { return __uint_as_float(w & 0xffff0000u); }
DI unsigned pk(float lo, float hi) { f32x2 v = {lo, hi}; return __builtin_bit_cast(unsigned, __builtin_convertvector(v, bf16x2)); }
DI u32x4 pk8(const f32x4& a, const f32x4& b) { return u32x4{pk(a[0], a[1]), pk(a[2], a[3]), pk(b[0], b[1]), pk(b[2], b[3])}; }
DI f32x4 mfma(const u32x4& a, const u32x4& b, const f32x4& c) {
    return __builtin_amdgcn_mfma_f32_16x16x32_bf16(__builtin_bit_cast(bf16x8, a), __builtin_bit_cast(bf16x8, b), c, 0, 0, 0);
}
DI int perm(int b, int i) { return 32 * (b >> 1) + 8 * (i >> 2) + 4 * (b & 1) + (i & 3); }
DI float gsum(float v) {
    auto a = __builtin_amdgcn_permlane16_swap(__float_as_uint(v), __float_as_uint(v), false, false);
    v = __uint_as_float(a[0]) + __uint_as_float(a[1]);
    auto b = __builtin_amdgcn_permlane32_swap(__float_as_uint(v), __float_as_uint(v), false, false);
    return __uint_as_float(b[0]) + __uint_as_float(b[1]);
}
DI float gmax(float v) {
    auto a = __builtin_amdgcn_permlane16_swap(__float_as_uint(v), __float_as_uint(v), false, false);
    v = fmaxf(__uint_as_float(a[0]), __uint_as_float(a[1]));
    auto b = __builtin_amdgcn_permlane32_swap(__float_as_uint(v), __float_as_uint(v), false, false);
    return fmaxf(__uint_as_float(b[0]), __uint_as_float(b[1]));
}
DI void reflect(f32x4 (&t)[4], int g, float scale) {
    float s = 0.f;
#pragma unroll
    for (int b = 0; b < 4; ++b) s += (t[b][0] + t[b][1]) + (t[b][2] + t[b][3]);
    s = gsum(s * 0.125f - (g == 3 ? t[3][3] : 0.f)) * (1.0f / 7.0f);
#pragma unroll
    for (int b = 0; b < 4; ++b)
#pragma unroll
        for (int r = 0; r < 4; ++r) t[b][r] = (t[b][r] - s) * scale;
    if (g == 3) t[3][3] = 0.f;
}
#define STAMP(k) do {} while (0)

#define STAMP_BASE 0
__global__ __launch_bounds__(320) void k_prep(const u16* __restrict__ x, const int* __restrict__ src, const int* __restrict__ dst,
                                              const u16* __restrict__ wm, const u16* __restrict__ wq, const u16* __restrict__ wk,
                                              const u16* __restrict__ wv, const u16* __restrict__ wo, char* __restrict__ ws) {
    __shared__ __attribute__((aligned(16))) unsigned sm[256 + 5 * 256 + EPC];
    const int bid = blockIdx.x, tid = threadIdx.x, lane = tid & 63, wave = __builtin_amdgcn_readfirstlane(tid >> 6);
    const int i = lane & 15, g = lane >> 4;
    STAMP(0);
    if (bid < NWG) {
        unsigned* hist = sm; unsigned* offs = sm + 256 + wave * 256; unsigned* sorted = sm + 256 + 5 * 256;
        const i32x4 d0 = *(const i32x4*)(dst + bid * EPC + 4 * tid), d1 = *(const i32x4*)(dst + bid * EPC + EPC / 2 + 4 * tid);
        const i32x4 s0 = *(const i32x4*)(src + bid * EPC + 4 * tid), s1 = *(const i32x4*)(src + bid * EPC + EPC / 2 + 4 * tid);
        if (tid < 256) hist[tid] = 0;
        u32x4* box = (u32x4*)(ws + OFF_BOX + bid * 3 * BOXB);
        box[tid] = u32x4{SENT, SENT, SENT, SENT};
        if (tid < 3 * BOXB / 16 - 320) box[320 + tid] = u32x4{SENT, SENT, SENT, SENT};
        __syncthreads();
        unsigned bk[8], rk[8], en[8];
#pragma unroll
        for (int k = 0; k < 8; ++k) {
            const unsigned d = k < 4 ? d0[k & 3] : d1[k & 3], s = k < 4 ? s0[k & 3] : s1[k & 3];
            bk[k] = (d * 52429u) >> 21;
            en[k] = s | (((d - bk[k] * NPB) * LROW) << 16);
            rk[k] = atomicAdd(&hist[bk[k]], 1u);
        }
        __syncthreads();
        const u32x4 c = *(const u32x4*)(hist + 4 * lane);
        const unsigned tot = c[0] + c[1] + c[2] + c[3];
        unsigned v = tot;
        v += __builtin_amdgcn_update_dpp(0u, v, 0x111, 0xf, 0xf, false);
        v += __builtin_amdgcn_update_dpp(0u, v, 0x112, 0xf, 0xf, false);
        v += __builtin_amdgcn_update_dpp(0u, v, 0x114, 0xf, 0xf, false);
        v += __builtin_amdgcn_update_dpp(0u, v, 0x118, 0xf, 0xf, false);
        v += __builtin_amdgcn_update_dpp(0u, v, 0x142, 0xa, 0xf, false);
        v += __builtin_amdgcn_update_dpp(0u, v, 0x143, 0xc, 0xf, false);
        const unsigned ex = v - tot;
        const u32x4 o = {ex, ex + c[0], ex + c[0] + c[1], ex + c[0] + c[1] + c[2]};
        *(u32x4*)(offs + 4 * lane) = o;
        if (wave == 0) *(u32x4*)(ws + OFF_TAB + bid * 1024 + lane * 16) = o | (c << 16);
        __builtin_amdgcn_wave_barrier();
#pragma unroll
        for (int k = 0; k < 8; ++k) sorted[offs[bk[k]] + rk[k]] = en[k];
        __syncthreads();
        u32x4* out = (u32x4*)(ws + OFF_SORT + bid * EPC * 4);
        out[tid] = *(const u32x4*)(sorted + 4 * tid);
        out[320 + tid] = *(const u32x4*)(sorted + 1280 + 4 * tid);
    } else if (bid < NWG + 125) {
        const int blk = (bid - NWG) * 5 + wave, row = blk * 16 + i;
        const u32x4 x0 = *(const u32x4*)(x + row * 64 + 8 * g), x1 = *(const u32x4*)(x + row * 64 + 32 + 8 * g);
        u32x4 qf[4][2], kf[4][2];
#pragma unroll
        for (int a = 0; a < 4; ++a)
#pragma unroll
            for (int s = 0; s < 2; ++s) {
                qf[a][s] = *(const u32x4*)(wq + perm(a, i) * 64 + 32 * s + 8 * g);
                kf[a][s] = *(const u32x4*)(wk + perm(a, i) * 64 + 32 * s + 8 * g);
            }
        f32x4 h[4];
#pragma unroll
        for (int j = 0; j < 2; ++j) {
            h[j] = f32x4{bflo(x0[2 * j]), bfhi(x0[2 * j]), bflo(x0[2 * j + 1]), bfhi(x0[2 * j + 1])};
            h[2 + j] = f32x4{bflo(x1[2 * j]), bfhi(x1[2 * j]), bflo(x1[2 * j + 1]), bfhi(x1[2 * j + 1])};
        }
        float s1 = 0.f;
#pragma unroll
        for (int b = 0; b < 4; ++b) s1 += (h[b][0] + h[b][1]) + (h[b][2] + h[b][3]);
        const float mean = gsum(s1) * (1.0f / 64.0f);
        float s2 = 0.f;
#pragma unroll
        for (int b = 0; b < 4; ++b) { h[b] -= mean; s2 += (h[b][0] * h[b][0] + h[b][1] * h[b][1]) + (h[b][2] * h[b][2] + h[b][3] * h[b][3]); }
        const float rstd = __builtin_amdgcn_rsqf(gsum(s2) * (1.0f / 64.0f) + 1e-5f);
        const float sh = gsum(g == 3 ? h[3][3] : 0.f) * rstd * (1.0f / 7.0f);
        f32x4 u[4];
#pragma unroll
        for (int b = 0; b < 4; ++b) { h[b] *= rstd; u[b] = h[b] + sh; }
        if (g == 3) u[3][3] = 1.0f;
        *(u32x4*)(ws + OFF_U + row * 128 + 16 * g) = pk8(u[0], u[1]);
        *(u32x4*)(ws + OFF_U + row * 128 + 64 + 16 * g) = pk8(u[2], u[3]);
        const u32x4 hf0 = pk8(h[0], h[1]), hf1 = pk8(h[2], h[3]);
        f32x4 t[4];
#pragma unroll
        for (int b = 0; b < 4; ++b) {
            f32x4 G[4];
#pragma unroll
            for (int a = 0; a < 4; ++a) G[a] = mfma(qf[a][1], kf[b][1], mfma(qf[a][0], kf[b][0], f32x4{}));
            t[b] = mfma(pk8(G[2], G[3]), hf1, mfma(pk8(G[0], G[1]), hf0, f32x4{}));
        }
        reflect(t, g, QSCALE);
        float n2 = 0.f;
#pragma unroll
        for (int b = 0; b < 4; ++b) n2 += (t[b][0] * t[b][0] + t[b][1] * t[b][1]) + (t[b][2] * t[b][2] + t[b][3] * t[b][3]);
        n2 = gsum(n2);
        if (g == 3 && !(n2 <= QN2_MAX)) t[3][3] = 1.0f;
        *(u32x4*)(ws + OFF_Q + blk * 2048 + lane * 16) = pk8(t[0], t[1]);
        *(u32x4*)(ws + OFF_Q + blk * 2048 + 1024 + lane * 16) = pk8(t[2], t[3]);
    } else if (wave < 4) {
        const int m = bid - (NWG + 125), eb = wave;
        const u16* w = m == 0 ? wv : wm;
        u32x4 bf[2];
#pragma unroll
        for (int s = 0; s < 2; ++s)
#pragma unroll
            for (int j = 0; j < 4; ++j)
                bf[s][j] = (unsigned)wo[(32 * s + 8 * g + 2 * j) * 64 + perm(eb, i)] | ((unsigned)wo[(32 * s + 8 * g + 2 * j + 1) * 64 + perm(eb, i)] << 16);
        f32x4 t[4];
#pragma unroll
        for (int a = 0; a < 4; ++a)
            t[a] = mfma(*(const u32x4*)(w + (16 * a + i) * 64 + 32 + 8 * g), bf[1], mfma(*(const u32x4*)(w + (16 * a + i) * 64 + 8 * g), bf[0], f32x4{}));
        reflect(t, g, 1.0f);
        *(u32x4*)(ws + OFF_W + ((m * 4 + eb) * 2) * 1024 + lane * 16) = pk8(t[0], t[1]);
        *(u32x4*)(ws + OFF_W + ((m * 4 + eb) * 2 + 1) * 1024 + lane * 16) = pk8(t[2], t[3]);
    }
    STAMP(1);
}
#undef STAMP_BASE

#define STAMP_BASE 4096
constexpr unsigned L_RING = 20480, L_LIST = 4 * L_RING, L_ZL = L_LIST + 10752, ZROW = 272, L_CTL = L_ZL + NPB * ZROW, L_TOT = L_CTL + 64;
constexpr int SEG = 24;

DI void hbar(LAS unsigned* ctr, unsigned& epoch, int lane) {
    epoch += 4;
    __builtin_amdgcn_fence(__ATOMIC_RELEASE, "workgroup");
    if (lane == 0) __hip_atomic_fetch_add(ctr, 1u, __ATOMIC_RELAXED, __HIP_MEMORY_SCOPE_WORKGROUP);
    while (__builtin_amdgcn_readfirstlane(__hip_atomic_load(ctr, __ATOMIC_RELAXED, __HIP_MEMORY_SCOPE_WORKGROUP)) < epoch) __builtin_amdgcn_s_sleep(2);
    __builtin_amdgcn_fence(__ATOMIC_ACQUIRE, "workgroup");
}
DI void fill_lists(char* lds, int hid, bool counters) {
    for (int k = hid; k < (int)(NPB * LROW / 8); k += 256) {
        const bool head = k % (LROW / 8) == 0;
        if (!head) *(u32x2*)(lds + L_LIST + 8 * k) = u32x2{SENT, SENT};
        else if (counters) *(u32x2*)(lds + L_LIST + 8 * k) = u32x2{8u, 0u};
    }
}

DI void attn_true_max(const char* __restrict__ ws, LAS char* red, int grp, int k0, int k1) {
    int lane;
    asm volatile("v_mbcnt_lo_u32_b32 %0, -1, 0\n\tv_mbcnt_hi_u32_b32 %0, -1, %0" : "=v"(lane));
    const int i = lane & 15, g = lane >> 4;
    const u16* U = (const u16*)(ws + OFF_U);
#pragma unroll 1
    for (int qb = 0; qb < 5; ++qb) {
        const u32x4 q0 = *(const u32x4*)(ws + OFF_Q + (grp * 5 + qb) * 2048 + lane * 16);
        u32x4 q1 = *(const u32x4*)(ws + OFF_Q + (grp * 5 + qb) * 2048 + 1024 + lane * 16);
        if (g == 3) q1[3] &= 0xffffu;
        float m = -INFINITY;
#pragma unroll 1
        for (int kb = 0; kb < NN; kb += 16) {
            const f32x4 s = mfma(*(const u32x4*)(U + (kb + i) * 64 + 32 + 8 * g), q1, mfma(*(const u32x4*)(U + (kb + i) * 64 + 8 * g), q0, f32x4{}));
            m = fmaxf(fmaxf(m, fmaxf(s[0], s[1])), fmaxf(s[2], s[3]));
        }
        m = gmax(m);
        f32x4 o[4] = {};
#pragma unroll 1
        for (int kb = k0; kb < k1; kb += 32) {
            const bool two = kb + 16 < k1;
            const int kc = two ? kb + 16 : kb;
            f32x4 pa = mfma(*(const u32x4*)(U + (kb + i) * 64 + 32 + 8 * g), q1, mfma(*(const u32x4*)(U + (kb + i) * 64 + 8 * g), q0, f32x4{}));
            f32x4 pb = mfma(*(const u32x4*)(U + (kc + i) * 64 + 32 + 8 * g), q1, mfma(*(const u32x4*)(U + (kc + i) * 64 + 8 * g), q0, f32x4{}));
#pragma unroll
            for (int r = 0; r < 4; ++r) { pa[r] = __builtin_amdgcn_exp2f(pa[r] - m); pb[r] = two ? __builtin_amdgcn_exp2f(pb[r] - m) : 0.f; }
            const u32x4 p = pk8(pa, pb);
#pragma unroll
            for (int db = 0; db < 4; ++db) {
                const u16* va = U + (kb + 4 * g) * 64 + 16 * db + i; const u16* vb = U + (kc + 4 * g) * 64 + 16 * db + i;
                const u32x4 vf = {(unsigned)va[0] | ((unsigned)va[64] << 16), (unsigned)va[128] | ((unsigned)va[192] << 16),
                                  (unsigned)vb[0] | ((unsigned)vb[64] << 16), (unsigned)vb[128] | ((unsigned)vb[192] << 16)};
                o[db] = mfma(vf, p, o[db]);
            }
        }
#pragma unroll
        for (int db = 0; db < 4; ++db) *(LAS f32x4*)(red + (qb * 4 + db) * 1024 + lane * 16) = o[db];
    }
}

__global__ __launch_bounds__(512, 2) void k_main(const u16* __restrict__ x, const u16* __restrict__ dinv, char* ws, u16* __restrict__ out) {
    __shared__ __attribute__((aligned(16))) char lds[L_TOT];
    const int bid = blockIdx.x, tid = threadIdx.x, lane = tid & 63, wave = __builtin_amdgcn_readfirstlane(tid >> 6);
    const int i = lane & 15, g = lane >> 4;
    const int grp = bid < 240 ? (bid >> 4) * 8 + (bid & 7) : 120 + ((bid - 240) >> 1), half = bid < 240 ? (bid >> 3) & 1 : bid & 1;
    const int me = 2 * grp + half;
    STAMP(0);
    if (wave < 4) {
        const int j = half * 4 + wave, k0 = j * 1280, k1 = j == 7 ? NN : k0 + 1280;
        const unsigned ring = (unsigned)(size_t)lds + wave * L_RING;
        const unsigned q4 = (lane & 15) >> 2, p4 = lane & 3, row = 4 * g + q4;
        const unsigned ka = ring + 128 * i + 16 * (g ^ (i & 6));
        const unsigned va = ring + 128 * row + 16 * ((p4 >> 1) ^ (row & 6)) + 8 * (p4 & 1);
        const unsigned dv = 128 * (lane >> 3) + 16 * ((lane & 7) ^ ((lane >> 3) & 6));
        const size_t ub = (size_t)(ws + OFF_U);
        const u32x4 srd = {(unsigned)ub, (unsigned)(ub >> 32) & 0xffffu, NN * 128u, 0x20000u};
        const char* qp = ws + OFF_Q + grp * 5 * 2048;
        int cnt = j == 7 ? 9 : 10; unsigned so = k0 * 128u, m0s, slow;
        asm volatile(
            "s_mov_b32 %[m0s], m0\n"
            "v_mov_b32 v18, %[dv]\n"
            "v_mov_b32 v19, %[lin]\n"
            "s_add_u32 m0, %[ring], 0\n"
            "s_nop 0\n"
            "buffer_load_dwordx4 v18, %[srd], %[so] offen offset:0 lds\n"
            "buffer_load_dwordx4 v18, %[srd], %[so] offen offset:1024 lds\n"
            "buffer_load_dwordx4 v18, %[srd], %[so] offen offset:2048 lds\n"
            "buffer_load_dwordx4 v18, %[srd], %[so] offen offset:3072 lds\n"
            "s_add_u32 %[so], %[so], 4096\n"
            "s_add_u32 m0, %[ring], 4096\n"
            "s_nop 0\n"
            "buffer_load_dwordx4 v18, %[srd], %[so] offen offset:0 lds\n"
            "buffer_load_dwordx4 v18, %[srd], %[so] offen offset:1024 lds\n"
            "buffer_load_dwordx4 v18, %[srd], %[so] offen offset:2048 lds\n"
            "buffer_load_dwordx4 v18, %[srd], %[so] offen offset:3072 lds\n"
            "s_add_u32 %[so], %[so], 4096\n"
            "v_add_u32 v144, 4096, v19\n"
            "v_add_u32 v145, 8192, v19\n"
            "global_load_dwordx4 v[24:27], v19, %[qp] offset:0\n"
            "global_load_dwordx4 v[28:31], v19, %[qp] offset:1024\n"
            "global_load_dwordx4 v[32:35], v19, %[qp] offset:2048\n"
            "global_load_dwordx4 v[36:39], v19, %[qp] offset:3072\n"
            "global_load_dwordx4 v[40:43], v144, %[qp] offset:0\n"
            "global_load_dwordx4 v[44:47], v144, %[qp] offset:1024\n"
            "global_load_dwordx4 v[48:51], v144, %[qp] offset:2048\n"
            "global_load_dwordx4 v[52:55], v144, %[qp] offset:3072\n"
            "global_load_dwordx4 v[56:59], v145, %[qp] offset:0\n"
            "global_load_dwordx4 v[60:63], v145, %[qp] offset:1024\n"
            "s_add_u32 m0, %[ring], 8192\n"
            "s_nop 0\n"
            "buffer_load_dwordx4 v18, %[srd], %[so] offen offset:0 lds\n"
            "buffer_load_dwordx4 v18, %[srd], %[so] offen offset:1024 lds\n"
            "buffer_load_dwordx4 v18, %[srd], %[so] offen offset:2048 lds\n"
            "buffer_load_dwordx4 v18, %[srd], %[so] offen offset:3072 lds\n"
            "s_add_u32 %[so], %[so], 4096\n"
            "s_add_u32 m0, %[ring], 12288\n"
            "s_nop 0\n"
            "buffer_load_dwordx4 v18, %[srd], %[so] offen offset:0 lds\n"
            "buffer_load_dwordx4 v18, %[srd], %[so] offen offset:1024 lds\n"
            "buffer_load_dwordx4 v18, %[srd], %[so] offen offset:2048 lds\n"
            "buffer_load_dwordx4 v18, %[srd], %[so] offen offset:3072 lds\n"
            "s_add_u32 %[so], %[so], 4096\n"
            "v_xor_b32 v16, 0, %[ka]\n"
            "v_xor_b32 v17, 64, %[ka]\n"
            "v_xor_b32 v20, 0, %[va]\n"
            "v_xor_b32 v21, 32, %[va]\n"
            "v_xor_b32 v22, 64, %[va]\n"
            "v_xor_b32 v23, 96, %[va]\n"
            "v_add_u32 v19, %[ring], v19\n"
            "s_barrier\n"
            "v_mov_b32 v64, 0\n"
            "v_mov_b32 v65, 0\n"
            "v_mov_b32 v66, 0\n"
            "v_mov_b32 v67, 0\n"
            "v_mov_b32 v68, 0\n"
            "v_mov_b32 v69, 0\n"
            "v_mov_b32 v70, 0\n"
            "v_mov_b32 v71, 0\n"
            "v_mov_b32 v72, 0\n"
            "v_mov_b32 v73, 0\n"
            "v_mov_b32 v74, 0\n"
            "v_mov_b32 v75, 0\n"
            "v_mov_b32 v76, 0\n"
            "v_mov_b32 v77, 0\n"
            "v_mov_b32 v78, 0\n"
            "v_mov_b32 v79, 0\n"
            "v_mov_b32 v80, 0\n"
            "v_mov_b32 v81, 0\n"
            "v_mov_b32 v82, 0\n"
            "v_mov_b32 v83, 0\n"
            "v_mov_b32 v84, 0\n"
            "v_mov_b32 v85, 0\n"
            "v_mov_b32 v86, 0\n"
            "v_mov_b32 v87, 0\n"
            "v_mov_b32 v88, 0\n"
            "v_mov_b32 v89, 0\n"
            "v_mov_b32 v90, 0\n"
            "v_mov_b32 v91, 0\n"
            "v_mov_b32 v92, 0\n"
            "v_mov_b32 v93, 0\n"
            "v_mov_b32 v94, 0\n"
            "v_mov_b32 v95, 0\n"
            "v_mov_b32 v96, 0\n"
            "v_mov_b32 v97, 0\n"
            "v_mov_b32 v98, 0\n"
            "v_mov_b32 v99, 0\n"
            "v_mov_b32 v100, 0\n"
            "v_mov_b32 v101, 0\n"
            "v_mov_b32 v102, 0\n"
            "v_mov_b32 v103, 0\n"
            "v_mov_b32 v104, 0\n"
            "v_mov_b32 v105, 0\n"
            "v_mov_b32 v106, 0\n"
            "v_mov_b32 v107, 0\n"
            "v_mov_b32 v108, 0\n"
            "v_mov_b32 v109, 0\n"
            "v_mov_b32 v110, 0\n"
            "v_mov_b32 v111, 0\n"
            "v_mov_b32 v112, 0\n"
            "v_mov_b32 v113, 0\n"
            "v_mov_b32 v114, 0\n"
            "v_mov_b32 v115, 0\n"
            "v_mov_b32 v116, 0\n"
            "v_mov_b32 v117, 0\n"
            "v_mov_b32 v118, 0\n"
            "v_mov_b32 v119, 0\n"
            "v_mov_b32 v120, 0\n"
            "v_mov_b32 v121, 0\n"
            "v_mov_b32 v122, 0\n"
            "v_mov_b32 v123, 0\n"
            "v_mov_b32 v124, 0\n"
            "v_mov_b32 v125, 0\n"
            "v_mov_b32 v126, 0\n"
            "v_mov_b32 v127, 0\n"
            "v_mov_b32 v128, 0\n"
            "v_mov_b32 v129, 0\n"
            "v_mov_b32 v130, 0\n"
            "v_mov_b32 v131, 0\n"
            "v_mov_b32 v132, 0\n"
            "v_mov_b32 v133, 0\n"
            "v_mov_b32 v134, 0\n"
            "v_mov_b32 v135, 0\n"
            "v_mov_b32 v136, 0\n"
            "v_mov_b32 v137, 0\n"
            "v_mov_b32 v138, 0\n"
            "v_mov_b32 v139, 0\n"
            "v_mov_b32 v140, 0\n"
            "v_mov_b32 v141, 0\n"
            "v_mov_b32 v142, 0\n"
            "v_mov_b32 v143, 0\n"
            "v_mov_b32 v184, 0\n"
            "v_mov_b32 v185, 0\n"
            "v_mov_b32 v186, 0\n"
            "v_mov_b32 v187, 0\n"
            "v_mov_b32 v188, 0\n"
            "v_mov_b32 v189, 0\n"
            "v_mov_b32 v190, 0\n"
            "v_mov_b32 v191, 0\n"
            "v_mov_b32 v174, 0\n"
            "v_mov_b32 v175, 0\n"
            "v_mov_b32 v224, 0\n"
            "v_mov_b32 v225, 0\n"
            "v_mov_b32 v226, 0\n"
            "v_mov_b32 v227, 0\n"
            "v_mov_b32 v228, 0\n"
            "v_mov_b32 v229, 0\n"
            "v_mov_b32 v230, 0\n"
            "v_mov_b32 v231, 0\n"
            "v_mov_b32 v232, 0\n"
            "v_mov_b32 v233, 0\n"
            "v_mov_b32 v234, 0\n"
            "v_mov_b32 v235, 0\n"
            "v_mov_b32 v236, 0\n"
            "v_mov_b32 v237, 0\n"
            "v_mov_b32 v238, 0\n"
            "v_mov_b32 v239, 0\n"
            "v_mov_b32 v240, 0\n"
            "v_mov_b32 v241, 0\n"
            "v_mov_b32 v242, 0\n"
            "v_mov_b32 v243, 0\n"
            "v_mov_b32 v244, 0\n"
            "v_mov_b32 v245, 0\n"
            "v_mov_b32 v246, 0\n"
            "v_mov_b32 v247, 0\n"
            "v_mov_b32 v248, 0\n"
            "v_mov_b32 v249, 0\n"
            "v_mov_b32 v250, 0\n"
            "v_mov_b32 v251, 0\n"
            "v_mov_b32 v252, 0\n"
            "v_mov_b32 v253, 0\n"
            "v_mov_b32 v254, 0\n"
            "v_mov_b32 v255, 0\n"
            "s_waitcnt vmcnt(8)\n"
            "ds_read_b128 v[192:195], v16 offset:0\n"
            "ds_read_b128 v[196:199], v17 offset:0\n"
            "ds_read_b128 v[200:203], v16 offset:2048\n"
            "ds_read_b128 v[204:207], v17 offset:2048\n"
            "ds_read_b128 v[208:211], v16 offset:4096\n"
            "ds_read_b128 v[212:215], v17 offset:4096\n"
            "ds_read_b128 v[216:219], v16 offset:6144\n"
            "ds_read_b128 v[220:223], v17 offset:6144\n"
            "v_or3_b32 v144, v31, v39, v47\n"
            "v_or3_b32 v144, v144, v55, v63\n"
            "v_cmp_lt_u32 vcc, 0xffff, v144\n"
            "s_nop 4\n"
            "s_lshr_b32 %[slow], vcc_hi, 16\n"
            "s_cbranch_scc1 8f\n"
            "s_mov_b64 vcc, -1\n"
            "s_waitcnt lgkmcnt(0)\n"
            "v_mfma_f32_16x16x32_bf16 v[144:147], v[192:195], v[24:27], 0\n"
            "v_mfma_f32_16x16x32_bf16 v[144:147], v[196:199], v[28:31], v[144:147]\n"
            "v_mfma_f32_16x16x32_bf16 v[148:151], v[200:203], v[24:27], 0\n"
            "v_mfma_f32_16x16x32_bf16 v[148:151], v[204:207], v[28:31], v[148:151]\n"
            "v_mfma_f32_16x16x32_bf16 v[152:155], v[208:211], v[24:27], 0\n"
            "v_mfma_f32_16x16x32_bf16 v[152:155], v[212:215], v[28:31], v[152:155]\n"
            "v_mfma_f32_16x16x32_bf16 v[156:159], v[216:219], v[24:27], 0\n"
            "v_mfma_f32_16x16x32_bf16 v[156:159], v[220:223], v[28:31], v[156:159]\n"
            "s_nop 1\n"
            ".p2align 6\n"
            "1:\n"
            "v_mfma_f32_16x16x32_bf16 v[160:163], v[192:195], v[32:35], 0\n"
            "v_exp_f32 v144, v144\n"
            "v_mfma_f32_16x16x32_bf16 v[160:163], v[196:199], v[36:39], v[160:163]\n"
            "v_exp_f32 v145, v145\n"
            "v_cvt_pk_bf16_f32 v191, v174, v175\n"
            "v_mfma_f32_16x16x32_bf16 v[164:167], v[200:203], v[32:35], 0\n"
            "v_exp_f32 v146, v146\n"
            "v_mfma_f32_16x16x32_bf16 v[164:167], v[204:207], v[36:39], v[164:167]\n"
            "v_exp_f32 v147, v147\n"
            "v_cvt_pk_bf16_f32 v176, v144, v145\n"
            "v_mfma_f32_16x16x32_bf16 v[168:171], v[208:211], v[32:35], 0\n"
            "v_exp_f32 v148, v148\n"
            "v_mfma_f32_16x16x32_bf16 v[168:171], v[212:215], v[36:39], v[168:171]\n"
            "v_exp_f32 v149, v149\n"
            "v_cvt_pk_bf16_f32 v177, v146, v147\n"
            "v_mfma_f32_16x16x32_bf16 v[172:175], v[216:219], v[32:35], 0\n"
            "v_exp_f32 v150, v150\n"
            "v_mfma_f32_16x16x32_bf16 v[172:175], v[220:223], v[36:39], v[172:175]\n"
            "v_exp_f32 v151, v151\n"
            "v_cvt_pk_bf16_f32 v178, v148, v149\n"
            "v_mfma_f32_16x16x32_bf16 v[128:131], v[224:227], v[184:187], v[128:131]\n"
            "v_exp_f32 v152, v152\n"
            "v_mfma_f32_16x16x32_bf16 v[128:131], v[228:231], v[188:191], v[128:131]\n"
            "v_exp_f32 v153, v153\n"
            "v_cvt_pk_bf16_f32 v179, v150, v151\n"
            "v_mfma_f32_16x16x32_bf16 v[132:135], v[236:239], v[188:191], v[132:135]\n"
            "v_exp_f32 v154, v154\n"
            "v_mfma_f32_16x16x32_bf16 v[132:135], v[232:235], v[184:187], v[132:135]\n"
            "ds_read_b64_tr_b16 v[224:225], v20 offset:0\n"
            "ds_read_b64_tr_b16 v[226:227], v20 offset:2048\n"
            "ds_read_b64_tr_b16 v[228:229], v20 offset:4096\n"
            "ds_read_b64_tr_b16 v[230:231], v20 offset:6144\n"
            "v_exp_f32 v155, v155\n"
            "v_cvt_pk_bf16_f32 v180, v152, v153\n"
            "v_mfma_f32_16x16x32_bf16 v[136:139], v[240:243], v[184:187], v[136:139]\n"
            "v_exp_f32 v156, v156\n"
            "v_mfma_f32_16x16x32_bf16 v[136:139], v[244:247], v[188:191], v[136:139]\n"
            "ds_read_b64_tr_b16 v[232:233], v21 offset:0\n"
            "ds_read_b64_tr_b16 v[234:235], v21 offset:2048\n"
            "ds_read_b64_tr_b16 v[236:237], v21 offset:4096\n"
            "ds_read_b64_tr_b16 v[238:239], v21 offset:6144\n"
            "v_exp_f32 v157, v157\n"
            "v_cvt_pk_bf16_f32 v181, v154, v155\n"
            "v_mfma_f32_16x16x32_bf16 v[140:143], v[252:255], v[188:191], v[140:143]\n"
            "v_exp_f32 v158, v158\n"
            "v_mfma_f32_16x16x32_bf16 v[140:143], v[248:251], v[184:187], v[140:143]\n"
            "ds_read_b64_tr_b16 v[240:241], v22 offset:0\n"
            "ds_read_b64_tr_b16 v[242:243], v22 offset:2048\n"
            "ds_read_b64_tr_b16 v[244:245], v22 offset:4096\n"
            "ds_read_b64_tr_b16 v[246:247], v22 offset:6144\n"
            "v_exp_f32 v159, v159\n"
            "v_cvt_pk_bf16_f32 v182, v156, v157\n"
            "v_mfma_f32_16x16x32_bf16 v[144:147], v[192:195], v[40:43], 0\n"
            "v_exp_f32 v160, v160\n"
            "v_mfma_f32_16x16x32_bf16 v[144:147], v[196:199], v[44:47], v[144:147]\n"
            "ds_read_b64_tr_b16 v[248:249], v23 offset:0\n"
            "ds_read_b64_tr_b16 v[250:251], v23 offset:2048\n"
            "ds_read_b64_tr_b16 v[252:253], v23 offset:4096\n"
            "ds_read_b64_tr_b16 v[254:255], v23 offset:6144\n"
            "v_exp_f32 v161, v161\n"
            "v_cvt_pk_bf16_f32 v183, v158, v159\n"
            "v_mfma_f32_16x16x32_bf16 v[148:151], v[200:203], v[40:43], 0\n"
            "v_exp_f32 v162, v162\n"
            "v_mfma_f32_16x16x32_bf16 v[148:151], v[204:207], v[44:47], v[148:151]\n"
            "v_exp_f32 v163, v163\n"
            "v_cvt_pk_bf16_f32 v184, v160, v161\n"
            "v_mfma_f32_16x16x32_bf16 v[152:155], v[208:211], v[40:43], 0\n"
            "v_exp_f32 v164, v164\n"
            "v_mfma_f32_16x16x32_bf16 v[152:155], v[212:215], v[44:47], v[152:155]\n"
            "v_exp_f32 v165, v165\n"
            "v_cvt_pk_bf16_f32 v185, v162, v163\n"
            "v_mfma_f32_16x16x32_bf16 v[156:159], v[216:219], v[40:43], 0\n"
            "v_exp_f32 v166, v166\n"
            "v_mfma_f32_16x16x32_bf16 v[156:159], v[220:223], v[44:47], v[156:159]\n"
            "s_waitcnt lgkmcnt(0)\n"
            "v_exp_f32 v167, v167\n"
            "v_cvt_pk_bf16_f32 v186, v164, v165\n"
            "v_mfma_f32_16x16x32_bf16 v[64:67], v[224:227], v[176:179], v[64:67]\n"
            "v_exp_f32 v168, v168\n"
            "v_mfma_f32_16x16x32_bf16 v[64:67], v[228:231], v[180:183], v[64:67]\n"
            "s_add_u32 m0, %[ring], 0\n"
            "s_cbranch_vccz 20f\n"
            "buffer_load_dwordx4 v18, %[srd], %[so] offen offset:0 lds\n"
            "buffer_load_dwordx4 v18, %[srd], %[so] offen offset:1024 lds\n"
            "buffer_load_dwordx4 v18, %[srd], %[so] offen offset:2048 lds\n"
            "buffer_load_dwordx4 v18, %[srd], %[so] offen offset:3072 lds\n"
            "s_add_u32 %[so], %[so], 4096\n"
            "30:\n"
            "v_exp_f32 v169, v169\n"
            "v_cvt_pk_bf16_f32 v187, v166, v167\n"
            "v_mfma_f32_16x16x32_bf16 v[68:71], v[236:239], v[180:183], v[68:71]\n"
            "v_exp_f32 v170, v170\n"
            "v_mfma_f32_16x16x32_bf16 v[68:71], v[232:235], v[176:179], v[68:71]\n"
            "v_exp_f32 v171, v171\n"
            "v_cvt_pk_bf16_f32 v188, v168, v169\n"
            "v_mfma_f32_16x16x32_bf16 v[72:75], v[240:243], v[176:179], v[72:75]\n"
            "v_exp_f32 v172, v172\n"
            "v_mfma_f32_16x16x32_bf16 v[72:75], v[244:247], v[180:183], v[72:75]\n"
            "v_exp_f32 v173, v173\n"
            "v_cvt_pk_bf16_f32 v189, v170, v171\n"
            "v_mfma_f32_16x16x32_bf16 v[76:79], v[252:255], v[180:183], v[76:79]\n"
            "v_exp_f32 v174, v174\n"
            "v_mfma_f32_16x16x32_bf16 v[76:79], v[248:251], v[176:179], v[76:79]\n"
            "v_exp_f32 v175, v175\n"
            "v_cvt_pk_bf16_f32 v190, v172, v173\n"
            "v_mfma_f32_16x16x32_bf16 v[160:163], v[192:195], v[48:51], 0\n"
            "v_exp_f32 v144, v144\n"
            "v_mfma_f32_16x16x32_bf16 v[160:163], v[196:199], v[52:55], v[160:163]\n"
            "s_add_u32 m0, %[ring], 4096\n"
            "s_cbranch_vccz 21f\n"
            "buffer_load_dwordx4 v18, %[srd], %[so] offen offset:0 lds\n"
            "buffer_load_dwordx4 v18, %[srd], %[so] offen offset:1024 lds\n"
            "buffer_load_dwordx4 v18, %[srd], %[so] offen offset:2048 lds\n"
            "buffer_load_dwordx4 v18, %[srd], %[so] offen offset:3072 lds\n"
            "s_add_u32 %[so], %[so], 4096\n"
            "31:\n"
            "v_exp_f32 v145, v145\n"
            "v_cvt_pk_bf16_f32 v191, v174, v175\n"
            "v_mfma_f32_16x16x32_bf16 v[164:167], v[200:203], v[48:51], 0\n"
            "v_exp_f32 v146, v146\n"
            "v_mfma_f32_16x16x32_bf16 v[164:167], v[204:207], v[52:55], v[164:167]\n"
            "v_exp_f32 v147, v147\n"
            "v_cvt_pk_bf16_f32 v176, v144, v145\n"
            "v_mfma_f32_16x16x32_bf16 v[168:171], v[208:211], v[48:51], 0\n"
            "v_exp_f32 v148, v148\n"
            "v_mfma_f32_16x16x32_bf16 v[168:171], v[212:215], v[52:55], v[168:171]\n"
            "v_exp_f32 v149, v149\n"
            "v_cvt_pk_bf16_f32 v177, v146, v147\n"
            "v_mfma_f32_16x16x32_bf16 v[172:175], v[216:219], v[48:51], 0\n"
            "v_exp_f32 v150, v150\n"
            "v_mfma_f32_16x16x32_bf16 v[172:175], v[220:223], v[52:55], v[172:175]\n"
            "v_exp_f32 v151, v151\n"
            "v_cvt_pk_bf16_f32 v178, v148, v149\n"
            "v_mfma_f32_16x16x32_bf16 v[80:83], v[224:227], v[184:187], v[80:83]\n"
            "v_exp_f32 v152, v152\n"
            "v_mfma_f32_16x16x32_bf16 v[80:83], v[228:231], v[188:191], v[80:83]\n"
            "v_exp_f32 v153, v153\n"
            "v_cvt_pk_bf16_f32 v179, v150, v151\n"
            "v_mfma_f32_16x16x32_bf16 v[84:87], v[236:239], v[188:191], v[84:87]\n"
            "v_exp_f32 v154, v154\n"
            "v_mfma_f32_16x16x32_bf16 v[84:87], v[232:235], v[184:187], v[84:87]\n"
            "v_exp_f32 v155, v155\n"
            "v_cvt_pk_bf16_f32 v180, v152, v153\n"
            "v_mfma_f32_16x16x32_bf16 v[88:91], v[240:243], v[184:187], v[88:91]\n"
            "v_exp_f32 v156, v156\n"
            "v_mfma_f32_16x16x32_bf16 v[88:91], v[244:247], v[188:191], v[88:91]\n"
            "v_exp_f32 v157, v157\n"
            "v_cvt_pk_bf16_f32 v181, v154, v155\n"
            "v_mfma_f32_16x16x32_bf16 v[92:95], v[252:255], v[188:191], v[92:95]\n"
            "v_exp_f32 v158, v158\n"
            "v_mfma_f32_16x16x32_bf16 v[92:95], v[248:251], v[184:187], v[92:95]\n"
            "v_exp_f32 v159, v159\n"
            "v_cvt_pk_bf16_f32 v182, v156, v157\n"
            "v_mfma_f32_16x16x32_bf16 v[144:147], v[192:195], v[56:59], 0\n"
            "v_exp_f32 v160, v160\n"
            "v_mfma_f32_16x16x32_bf16 v[144:147], v[196:199], v[60:63], v[144:147]\n"
            "s_waitcnt vmcnt(8)\n"
            "v_exp_f32 v161, v161\n"
            "v_cvt_pk_bf16_f32 v183, v158, v159\n"
            "v_mfma_f32_16x16x32_bf16 v[148:151], v[200:203], v[56:59], 0\n"
            "v_exp_f32 v162, v162\n"
            "v_mfma_f32_16x16x32_bf16 v[148:151], v[204:207], v[60:63], v[148:151]\n"
            "ds_read_b128 v[192:195], v16 offset:8192\n"
            "ds_read_b128 v[196:199], v17 offset:8192\n"
            "v_exp_f32 v163, v163\n"
            "v_cvt_pk_bf16_f32 v184, v160, v161\n"
            "v_mfma_f32_16x16x32_bf16 v[152:155], v[208:211], v[56:59], 0\n"
            "v_exp_f32 v164, v164\n"
            "v_mfma_f32_16x16x32_bf16 v[152:155], v[212:215], v[60:63], v[152:155]\n"
            "ds_read_b128 v[200:203], v16 offset:10240\n"
            "ds_read_b128 v[204:207], v17 offset:10240\n"
            "v_exp_f32 v165, v165\n"
            "v_cvt_pk_bf16_f32 v185, v162, v163\n"
            "v_mfma_f32_16x16x32_bf16 v[156:159], v[216:219], v[56:59], 0\n"
            "v_exp_f32 v166, v166\n"
            "v_mfma_f32_16x16x32_bf16 v[156:159], v[220:223], v[60:63], v[156:159]\n"
            "ds_read_b128 v[208:211], v16 offset:12288\n"
            "ds_read_b128 v[212:215], v17 offset:12288\n"
            "v_exp_f32 v167, v167\n"
            "v_cvt_pk_bf16_f32 v186, v164, v165\n"
            "v_mfma_f32_16x16x32_bf16 v[96:99], v[224:227], v[176:179], v[96:99]\n"
            "v_exp_f32 v168, v168\n"
            "v_mfma_f32_16x16x32_bf16 v[96:99], v[228:231], v[180:183], v[96:99]\n"
            "ds_read_b128 v[216:219], v16 offset:14336\n"
            "ds_read_b128 v[220:223], v17 offset:14336\n"
            "v_exp_f32 v169, v169\n"
            "v_cvt_pk_bf16_f32 v187, v166, v167\n"
            "v_mfma_f32_16x16x32_bf16 v[100:103], v[236:239], v[180:183], v[100:103]\n"
            "v_exp_f32 v170, v170\n"
            "v_mfma_f32_16x16x32_bf16 v[100:103], v[232:235], v[176:179], v[100:103]\n"
            "v_exp_f32 v171, v171\n"
            "v_cvt_pk_bf16_f32 v188, v168, v169\n"
            "v_mfma_f32_16x16x32_bf16 v[104:107], v[240:243], v[176:179], v[104:107]\n"
            "v_exp_f32 v172, v172\n"
            "v_mfma_f32_16x16x32_bf16 v[104:107], v[244:247], v[180:183], v[104:107]\n"
            "v_exp_f32 v173, v173\n"
            "v_cvt_pk_bf16_f32 v189, v170, v171\n"
            "v_mfma_f32_16x16x32_bf16 v[108:111], v[252:255], v[180:183], v[108:111]\n"
            "v_exp_f32 v174, v174\n"
            "v_mfma_f32_16x16x32_bf16 v[108:111], v[248:251], v[176:179], v[108:111]\n"
            "s_waitcnt lgkmcnt(0)\n"
            "v_exp_f32 v175, v175\n"
            "v_cvt_pk_bf16_f32 v190, v172, v173\n"
            "v_mfma_f32_16x16x32_bf16 v[160:163], v[192:195], v[24:27], 0\n"
            "v_exp_f32 v144, v144\n"
            "v_mfma_f32_16x16x32_bf16 v[160:163], v[196:199], v[28:31], v[160:163]\n"
            "v_exp_f32 v145, v145\n"
            "v_cvt_pk_bf16_f32 v191, v174, v175\n"
            "v_mfma_f32_16x16x32_bf16 v[164:167], v[200:203], v[24:27], 0\n"
            "v_exp_f32 v146, v146\n"
            "v_mfma_f32_16x16x32_bf16 v[164:167], v[204:207], v[28:31], v[164:167]\n"
            "v_exp_f32 v147, v147\n"
            "v_cvt_pk_bf16_f32 v176, v144, v145\n"
            "v_mfma_f32_16x16x32_bf16 v[168:171], v[208:211], v[24:27], 0\n"
            "v_exp_f32 v148, v148\n"
            "v_mfma_f32_16x16x32_bf16 v[168:171], v[212:215], v[28:31], v[168:171]\n"
            "v_exp_f32 v149, v149\n"
            "v_cvt_pk_bf16_f32 v177, v146, v147\n"
            "v_mfma_f32_16x16x32_bf16 v[172:175], v[216:219], v[24:27], 0\n"
            "v_exp_f32 v150, v150\n"
            "v_mfma_f32_16x16x32_bf16 v[172:175], v[220:223], v[28:31], v[172:175]\n"
            "v_exp_f32 v151, v151\n"
            "v_cvt_pk_bf16_f32 v178, v148, v149\n"
            "v_mfma_f32_16x16x32_bf16 v[112:115], v[224:227], v[184:187], v[112:115]\n"
            "v_exp_f32 v152, v152\n"
            "v_mfma_f32_16x16x32_bf16 v[112:115], v[228:231], v[188:191], v[112:115]\n"
            "v_exp_f32 v153, v153\n"
            "v_cvt_pk_bf16_f32 v179, v150, v151\n"
            "v_mfma_f32_16x16x32_bf16 v[116:119], v[236:239], v[188:191], v[116:119]\n"
            "v_exp_f32 v154, v154\n"
            "v_mfma_f32_16x16x32_bf16 v[116:119], v[232:235], v[184:187], v[116:119]\n"
            "v_exp_f32 v155, v155\n"
            "v_cvt_pk_bf16_f32 v180, v152, v153\n"
            "v_mfma_f32_16x16x32_bf16 v[120:123], v[240:243], v[184:187], v[120:123]\n"
            "v_exp_f32 v156, v156\n"
            "v_mfma_f32_16x16x32_bf16 v[120:123], v[244:247], v[188:191], v[120:123]\n"
            "v_exp_f32 v157, v157\n"
            "v_cvt_pk_bf16_f32 v181, v154, v155\n"
            "v_mfma_f32_16x16x32_bf16 v[124:127], v[252:255], v[188:191], v[124:127]\n"
            "v_exp_f32 v158, v158\n"
            "v_mfma_f32_16x16x32_bf16 v[124:127], v[248:251], v[184:187], v[124:127]\n"
            "v_exp_f32 v159, v159\n"
            "v_cvt_pk_bf16_f32 v182, v156, v157\n"
            "v_mfma_f32_16x16x32_bf16 v[144:147], v[192:195], v[32:35], 0\n"
            "v_exp_f32 v160, v160\n"
            "v_mfma_f32_16x16x32_bf16 v[144:147], v[196:199], v[36:39], v[144:147]\n"
            "v_exp_f32 v161, v161\n"
            "v_cvt_pk_bf16_f32 v183, v158, v159\n"
            "v_mfma_f32_16x16x32_bf16 v[148:151], v[200:203], v[32:35], 0\n"
            "v_exp_f32 v162, v162\n"
            "v_mfma_f32_16x16x32_bf16 v[148:151], v[204:207], v[36:39], v[148:151]\n"
            "v_exp_f32 v163, v163\n"
            "v_cvt_pk_bf16_f32 v184, v160, v161\n"
            "v_mfma_f32_16x16x32_bf16 v[152:155], v[208:211], v[32:35], 0\n"
            "v_exp_f32 v164, v164\n"
            "v_mfma_f32_16x16x32_bf16 v[152:155], v[212:215], v[36:39], v[152:155]\n"
            "v_exp_f32 v165, v165\n"
            "v_cvt_pk_bf16_f32 v185, v162, v163\n"
            "v_mfma_f32_16x16x32_bf16 v[156:159], v[216:219], v[32:35], 0\n"
            "v_exp_f32 v166, v166\n"
            "v_mfma_f32_16x16x32_bf16 v[156:159], v[220:223], v[36:39], v[156:159]\n"
            "v_exp_f32 v167, v167\n"
            "v_cvt_pk_bf16_f32 v186, v164, v165\n"
            "v_mfma_f32_16x16x32_bf16 v[128:131], v[224:227], v[176:179], v[128:131]\n"
            "v_exp_f32 v168, v168\n"
            "v_mfma_f32_16x16x32_bf16 v[128:131], v[228:231], v[180:183], v[128:131]\n"
            "v_exp_f32 v169, v169\n"
            "v_cvt_pk_bf16_f32 v187, v166, v167\n"
            "v_mfma_f32_16x16x32_bf16 v[132:135], v[236:239], v[180:183], v[132:135]\n"
            "v_exp_f32 v170, v170\n"
            "v_mfma_f32_16x16x32_bf16 v[132:135], v[232:235], v[176:179], v[132:135]\n"
            "ds_read_b64_tr_b16 v[224:225], v20 offset:8192\n"
            "ds_read_b64_tr_b16 v[226:227], v20 offset:10240\n"
            "ds_read_b64_tr_b16 v[228:229], v20 offset:12288\n"
            "ds_read_b64_tr_b16 v[230:231], v20 offset:14336\n"
            "v_exp_f32 v171, v171\n"
            "v_cvt_pk_bf16_f32 v188, v168, v169\n"
            "v_mfma_f32_16x16x32_bf16 v[136:139], v[240:243], v[176:179], v[136:139]\n"
            "v_exp_f32 v172, v172\n"
            "v_mfma_f32_16x16x32_bf16 v[136:139], v[244:247], v[180:183], v[136:139]\n"
            "ds_read_b64_tr_b16 v[232:233], v21 offset:8192\n"
            "ds_read_b64_tr_b16 v[234:235], v21 offset:10240\n"
            "ds_read_b64_tr_b16 v[236:237], v21 offset:12288\n"
            "ds_read_b64_tr_b16 v[238:239], v21 offset:14336\n"
            "v_exp_f32 v173, v173\n"
            "v_cvt_pk_bf16_f32 v189, v170, v171\n"
            "v_mfma_f32_16x16x32_bf16 v[140:143], v[252:255], v[180:183], v[140:143]\n"
            "v_exp_f32 v174, v174\n"
            "v_mfma_f32_16x16x32_bf16 v[140:143], v[248:251], v[176:179], v[140:143]\n"
            "ds_read_b64_tr_b16 v[240:241], v22 offset:8192\n"
            "ds_read_b64_tr_b16 v[242:243], v22 offset:10240\n"
            "ds_read_b64_tr_b16 v[244:245], v22 offset:12288\n"
            "ds_read_b64_tr_b16 v[246:247], v22 offset:14336\n"
            "v_exp_f32 v175, v175\n"
            "v_cvt_pk_bf16_f32 v190, v172, v173\n"
            "v_mfma_f32_16x16x32_bf16 v[160:163], v[192:195], v[40:43], 0\n"
            "v_exp_f32 v144, v144\n"
            "v_mfma_f32_16x16x32_bf16 v[160:163], v[196:199], v[44:47], v[160:163]\n"
            "ds_read_b64_tr_b16 v[248:249], v23 offset:8192\n"
            "ds_read_b64_tr_b16 v[250:251], v23 offset:10240\n"
            "ds_read_b64_tr_b16 v[252:253], v23 offset:12288\n"
            "ds_read_b64_tr_b16 v[254:255], v23 offset:14336\n"
            "v_exp_f32 v145, v145\n"
            "v_cvt_pk_bf16_f32 v191, v174, v175\n"
            "v_mfma_f32_16x16x32_bf16 v[164:167], v[200:203], v[40:43], 0\n"
            "v_exp_f32 v146, v146\n"
            "v_mfma_f32_16x16x32_bf16 v[164:167], v[204:207], v[44:47], v[164:167]\n"
            "v_exp_f32 v147, v147\n"
            "v_cvt_pk_bf16_f32 v176, v144, v145\n"
            "v_mfma_f32_16x16x32_bf16 v[168:171], v[208:211], v[40:43], 0\n"
            "v_exp_f32 v148, v148\n"
            "v_mfma_f32_16x16x32_bf16 v[168:171], v[212:215], v[44:47], v[168:171]\n"
            "v_exp_f32 v149, v149\n"
            "v_cvt_pk_bf16_f32 v177, v146, v147\n"
            "v_mfma_f32_16x16x32_bf16 v[172:175], v[216:219], v[40:43], 0\n"
            "v_exp_f32 v150, v150\n"
            "v_mfma_f32_16x16x32_bf16 v[172:175], v[220:223], v[44:47], v[172:175]\n"
            "s_waitcnt lgkmcnt(0)\n"
            "v_exp_f32 v151, v151\n"
            "v_cvt_pk_bf16_f32 v178, v148, v149\n"
            "v_mfma_f32_16x16x32_bf16 v[64:67], v[224:227], v[184:187], v[64:67]\n"
            "v_exp_f32 v152, v152\n"
            "v_mfma_f32_16x16x32_bf16 v[64:67], v[228:231], v[188:191], v[64:67]\n"
            "s_add_u32 m0, %[ring], 8192\n"
            "s_cbranch_vccz 22f\n"
            "buffer_load_dwordx4 v18, %[srd], %[so] offen offset:0 lds\n"
            "buffer_load_dwordx4 v18, %[srd], %[so] offen offset:1024 lds\n"
            "buffer_load_dwordx4 v18, %[srd], %[so] offen offset:2048 lds\n"
            "buffer_load_dwordx4 v18, %[srd], %[so] offen offset:3072 lds\n"
            "s_add_u32 %[so], %[so], 4096\n"
            "32:\n"
            "v_exp_f32 v153, v153\n"
            "v_cvt_pk_bf16_f32 v179, v150, v151\n"
            "v_mfma_f32_16x16x32_bf16 v[68:71], v[236:239], v[188:191], v[68:71]\n"
            "v_exp_f32 v154, v154\n"
            "v_mfma_f32_16x16x32_bf16 v[68:71], v[232:235], v[184:187], v[68:71]\n"
            "v_exp_f32 v155, v155\n"
            "v_cvt_pk_bf16_f32 v180, v152, v153\n"
            "v_mfma_f32_16x16x32_bf16 v[72:75], v[240:243], v[184:187], v[72:75]\n"
            "v_exp_f32 v156, v156\n"
            "v_mfma_f32_16x16x32_bf16 v[72:75], v[244:247], v[188:191], v[72:75]\n"
            "v_exp_f32 v157, v157\n"
            "v_cvt_pk_bf16_f32 v181, v154, v155\n"
            "v_mfma_f32_16x16x32_bf16 v[76:79], v[252:255], v[188:191], v[76:79]\n"
            "v_exp_f32 v158, v158\n"
            "v_mfma_f32_16x16x32_bf16 v[76:79], v[248:251], v[184:187], v[76:79]\n"
            "v_exp_f32 v159, v159\n"
            "v_cvt_pk_bf16_f32 v182, v156, v157\n"
            "v_mfma_f32_16x16x32_bf16 v[144:147], v[192:195], v[48:51], 0\n"
            "v_exp_f32 v160, v160\n"
            "v_mfma_f32_16x16x32_bf16 v[144:147], v[196:199], v[52:55], v[144:147]\n"
            "s_add_u32 m0, %[ring], 12288\n"
            "s_cbranch_vccz 23f\n"
            "buffer_load_dwordx4 v18, %[srd], %[so] offen offset:0 lds\n"
            "buffer_load_dwordx4 v18, %[srd], %[so] offen offset:1024 lds\n"
            "buffer_load_dwordx4 v18, %[srd], %[so] offen offset:2048 lds\n"
            "buffer_load_dwordx4 v18, %[srd], %[so] offen offset:3072 lds\n"
            "s_add_u32 %[so], %[so], 4096\n"
            "33:\n"
            "v_exp_f32 v161, v161\n"
            "v_cvt_pk_bf16_f32 v183, v158, v159\n"
            "v_mfma_f32_16x16x32_bf16 v[148:151], v[200:203], v[48:51], 0\n"
            "v_exp_f32 v162, v162\n"
            "v_mfma_f32_16x16x32_bf16 v[148:151], v[204:207], v[52:55], v[148:151]\n"
            "v_exp_f32 v163, v163\n"
            "v_cvt_pk_bf16_f32 v184, v160, v161\n"
            "v_mfma_f32_16x16x32_bf16 v[152:155], v[208:211], v[48:51], 0\n"
            "v_exp_f32 v164, v164\n"
            "v_mfma_f32_16x16x32_bf16 v[152:155], v[212:215], v[52:55], v[152:155]\n"
            "v_exp_f32 v165, v165\n"
            "v_cvt_pk_bf16_f32 v185, v162, v163\n"
            "v_mfma_f32_16x16x32_bf16 v[156:159], v[216:219], v[48:51], 0\n"
            "v_exp_f32 v166, v166\n"
            "v_mfma_f32_16x16x32_bf16 v[156:159], v[220:223], v[52:55], v[156:159]\n"
            "v_exp_f32 v167, v167\n"
            "v_cvt_pk_bf16_f32 v186, v164, v165\n"
            "v_mfma_f32_16x16x32_bf16 v[80:83], v[224:227], v[176:179], v[80:83]\n"
            "v_exp_f32 v168, v168\n"
            "v_mfma_f32_16x16x32_bf16 v[80:83], v[228:231], v[180:183], v[80:83]\n"
            "v_exp_f32 v169, v169\n"
            "v_cvt_pk_bf16_f32 v187, v166, v167\n"
            "v_mfma_f32_16x16x32_bf16 v[84:87], v[236:239], v[180:183], v[84:87]\n"
            "v_exp_f32 v170, v170\n"
            "v_mfma_f32_16x16x32_bf16 v[84:87], v[232:235], v[176:179], v[84:87]\n"
            "v_exp_f32 v171, v171\n"
            "v_cvt_pk_bf16_f32 v188, v168, v169\n"
            "v_mfma_f32_16x16x32_bf16 v[88:91], v[240:243], v[176:179], v[88:91]\n"
            "v_exp_f32 v172, v172\n"
            "v_mfma_f32_16x16x32_bf16 v[88:91], v[244:247], v[180:183], v[88:91]\n"
            "v_exp_f32 v173, v173\n"
            "v_cvt_pk_bf16_f32 v189, v170, v171\n"
            "v_mfma_f32_16x16x32_bf16 v[92:95], v[252:255], v[180:183], v[92:95]\n"
            "v_exp_f32 v174, v174\n"
            "v_mfma_f32_16x16x32_bf16 v[92:95], v[248:251], v[176:179], v[92:95]\n"
            "v_exp_f32 v175, v175\n"
            "v_cvt_pk_bf16_f32 v190, v172, v173\n"
            "v_mfma_f32_16x16x32_bf16 v[160:163], v[192:195], v[56:59], 0\n"
            "v_exp_f32 v144, v144\n"
            "v_mfma_f32_16x16x32_bf16 v[160:163], v[196:199], v[60:63], v[160:163]\n"
            "s_waitcnt vmcnt(8)\n"
            "v_exp_f32 v145, v145\n"
            "v_cvt_pk_bf16_f32 v191, v174, v175\n"
            "v_mfma_f32_16x16x32_bf16 v[164:167], v[200:203], v[56:59], 0\n"
            "v_exp_f32 v146, v146\n"
            "v_mfma_f32_16x16x32_bf16 v[164:167], v[204:207], v[60:63], v[164:167]\n"
            "ds_read_b128 v[192:195], v16 offset:0\n"
            "ds_read_b128 v[196:199], v17 offset:0\n"
            "v_exp_f32 v147, v147\n"
            "v_cvt_pk_bf16_f32 v176, v144, v145\n"
            "v_mfma_f32_16x16x32_bf16 v[168:171], v[208:211], v[56:59], 0\n"
            "v_exp_f32 v148, v148\n"
            "v_mfma_f32_16x16x32_bf16 v[168:171], v[212:215], v[60:63], v[168:171]\n"
            "ds_read_b128 v[200:203], v16 offset:2048\n"
            "ds_read_b128 v[204:207], v17 offset:2048\n"
            "v_exp_f32 v149, v149\n"
            "v_cvt_pk_bf16_f32 v177, v146, v147\n"
            "v_mfma_f32_16x16x32_bf16 v[172:175], v[216:219], v[56:59], 0\n"
            "v_exp_f32 v150, v150\n"
            "v_mfma_f32_16x16x32_bf16 v[172:175], v[220:223], v[60:63], v[172:175]\n"
            "ds_read_b128 v[208:211], v16 offset:4096\n"
            "ds_read_b128 v[212:215], v17 offset:4096\n"
            "v_exp_f32 v151, v151\n"
            "v_cvt_pk_bf16_f32 v178, v148, v149\n"
            "v_mfma_f32_16x16x32_bf16 v[96:99], v[224:227], v[184:187], v[96:99]\n"
            "v_exp_f32 v152, v152\n"
            "v_mfma_f32_16x16x32_bf16 v[96:99], v[228:231], v[188:191], v[96:99]\n"
            "ds_read_b128 v[216:219], v16 offset:6144\n"
            "ds_read_b128 v[220:223], v17 offset:6144\n"
            "v_exp_f32 v153, v153\n"
            "v_cvt_pk_bf16_f32 v179, v150, v151\n"
            "v_mfma_f32_16x16x32_bf16 v[100:103], v[236:239], v[188:191], v[100:103]\n"
            "v_exp_f32 v154, v154\n"
            "v_mfma_f32_16x16x32_bf16 v[100:103], v[232:235], v[184:187], v[100:103]\n"
            "v_exp_f32 v155, v155\n"
            "v_cvt_pk_bf16_f32 v180, v152, v153\n"
            "v_mfma_f32_16x16x32_bf16 v[104:107], v[240:243], v[184:187], v[104:107]\n"
            "v_exp_f32 v156, v156\n"
            "v_mfma_f32_16x16x32_bf16 v[104:107], v[244:247], v[188:191], v[104:107]\n"
            "v_exp_f32 v157, v157\n"
            "v_cvt_pk_bf16_f32 v181, v154, v155\n"
            "v_mfma_f32_16x16x32_bf16 v[108:111], v[252:255], v[188:191], v[108:111]\n"
            "v_exp_f32 v158, v158\n"
            "v_mfma_f32_16x16x32_bf16 v[108:111], v[248:251], v[184:187], v[108:111]\n"
            "s_waitcnt lgkmcnt(0)\n"
            "v_exp_f32 v159, v159\n"
            "v_cvt_pk_bf16_f32 v182, v156, v157\n"
            "v_mfma_f32_16x16x32_bf16 v[144:147], v[192:195], v[24:27], 0\n"
            "v_exp_f32 v160, v160\n"
            "v_mfma_f32_16x16x32_bf16 v[144:147], v[196:199], v[28:31], v[144:147]\n"
            "v_exp_f32 v161, v161\n"
            "v_cvt_pk_bf16_f32 v183, v158, v159\n"
            "v_mfma_f32_16x16x32_bf16 v[148:151], v[200:203], v[24:27], 0\n"
            "v_exp_f32 v162, v162\n"
            "v_mfma_f32_16x16x32_bf16 v[148:151], v[204:207], v[28:31], v[148:151]\n"
            "v_exp_f32 v163, v163\n"
            "v_cvt_pk_bf16_f32 v184, v160, v161\n"
            "v_mfma_f32_16x16x32_bf16 v[152:155], v[208:211], v[24:27], 0\n"
            "v_exp_f32 v164, v164\n"
            "v_mfma_f32_16x16x32_bf16 v[152:155], v[212:215], v[28:31], v[152:155]\n"
            "v_exp_f32 v165, v165\n"
            "v_cvt_pk_bf16_f32 v185, v162, v163\n"
            "v_mfma_f32_16x16x32_bf16 v[156:159], v[216:219], v[24:27], 0\n"
            "v_exp_f32 v166, v166\n"
            "v_mfma_f32_16x16x32_bf16 v[156:159], v[220:223], v[28:31], v[156:159]\n"
            "v_exp_f32 v167, v167\n"
            "v_cvt_pk_bf16_f32 v186, v164, v165\n"
            "v_mfma_f32_16x16x32_bf16 v[112:115], v[224:227], v[176:179], v[112:115]\n"
            "v_exp_f32 v168, v168\n"
            "v_mfma_f32_16x16x32_bf16 v[112:115], v[228:231], v[180:183], v[112:115]\n"
            "s_sub_u32 %[cnt], %[cnt], 1\n"
            "v_exp_f32 v169, v169\n"
            "v_cvt_pk_bf16_f32 v187, v166, v167\n"
            "v_mfma_f32_16x16x32_bf16 v[116:119], v[236:239], v[180:183], v[116:119]\n"
            "v_exp_f32 v170, v170\n"
            "v_mfma_f32_16x16x32_bf16 v[116:119], v[232:235], v[176:179], v[116:119]\n"
            "s_cmp_gt_u32 %[cnt], 1\n"
            "s_cselect_b64 vcc, -1, 0\n"
            "v_exp_f32 v171, v171\n"
            "v_cvt_pk_bf16_f32 v188, v168, v169\n"
            "v_mfma_f32_16x16x32_bf16 v[120:123], v[240:243], v[176:179], v[120:123]\n"
            "v_exp_f32 v172, v172\n"
            "v_mfma_f32_16x16x32_bf16 v[120:123], v[244:247], v[180:183], v[120:123]\n"
            "s_cmp_lg_u32 %[cnt], 0\n"
            "v_exp_f32 v173, v173\n"
            "v_cvt_pk_bf16_f32 v189, v170, v171\n"
            "v_mfma_f32_16x16x32_bf16 v[124:127], v[252:255], v[180:183], v[124:127]\n"
            "v_exp_f32 v174, v174\n"
            "v_mfma_f32_16x16x32_bf16 v[124:127], v[248:251], v[176:179], v[124:127]\n"
            "v_exp_f32 v175, v175\n"
            "v_cvt_pk_bf16_f32 v190, v172, v173\n"
            "s_cbranch_scc1 1b\n"
            "v_cvt_pk_bf16_f32 v191, v174, v175\n"
            "ds_write_b128 v19, v[64:67] offset:0\n"
            "ds_write_b128 v19, v[68:71] offset:1024\n"
            "v_mfma_f32_16x16x32_bf16 v[128:131], v[224:227], v[184:187], v[128:131]\n"
            "ds_write_b128 v19, v[72:75] offset:2048\n"
            "v_mfma_f32_16x16x32_bf16 v[128:131], v[228:231], v[188:191], v[128:131]\n"
            "ds_write_b128 v19, v[76:79] offset:3072\n"
            "v_mfma_f32_16x16x32_bf16 v[132:135], v[232:235], v[184:187], v[132:135]\n"
            "ds_write_b128 v19, v[80:83] offset:4096\n"
            "v_mfma_f32_16x16x32_bf16 v[132:135], v[236:239], v[188:191], v[132:135]\n"
            "ds_write_b128 v19, v[84:87] offset:5120\n"
            "v_mfma_f32_16x16x32_bf16 v[136:139], v[240:243], v[184:187], v[136:139]\n"
            "ds_write_b128 v19, v[88:91] offset:6144\n"
            "v_mfma_f32_16x16x32_bf16 v[136:139], v[244:247], v[188:191], v[136:139]\n"
            "ds_write_b128 v19, v[92:95] offset:7168\n"
            "v_mfma_f32_16x16x32_bf16 v[140:143], v[248:251], v[184:187], v[140:143]\n"
            "ds_write_b128 v19, v[96:99] offset:8192\n"
            "v_mfma_f32_16x16x32_bf16 v[140:143], v[252:255], v[188:191], v[140:143]\n"
            "ds_write_b128 v19, v[100:103] offset:9216\n"
            "ds_write_b128 v19, v[104:107] offset:10240\n"
            "ds_write_b128 v19, v[108:111] offset:11264\n"
            "ds_write_b128 v19, v[112:115] offset:12288\n"
            "ds_write_b128 v19, v[116:119] offset:13312\n"
            "ds_write_b128 v19, v[120:123] offset:14336\n"
            "ds_write_b128 v19, v[124:127] offset:15360\n"
            "s_nop 1\n"
            "ds_write_b128 v19, v[128:131] offset:16384\n"
            "ds_write_b128 v19, v[132:135] offset:17408\n"
            "ds_write_b128 v19, v[136:139] offset:18432\n"
            "ds_write_b128 v19, v[140:143] offset:19456\n"
            "s_branch 9f\n"
            "20:\n"
            "s_waitcnt vmcnt(0)\n"
            "s_branch 30b\n"
            "21:\n"
            "s_waitcnt vmcnt(0)\n"
            "s_branch 31b\n"
            "22:\n"
            "s_waitcnt vmcnt(0)\n"
            "s_branch 32b\n"
            "23:\n"
            "s_waitcnt vmcnt(0)\n"
            "s_branch 33b\n"
            "8:\n"
            "s_waitcnt vmcnt(0)\n"
            "9:\n"
            "s_waitcnt lgkmcnt(0)\n"
            "s_mov_b32 m0, %[m0s]\n"
            : [cnt] "+s"(cnt), [so] "+s"(so), [m0s] "=&s"(m0s), [slow] "=&s"(slow)
            : [ka] "v"(ka), [va] "v"(va), [dv] "v"(dv), [lin] "v"(lane * 16u), [srd] "s"(srd), [qp] "s"(qp), [ring] "s"(ring)
            : "scc", "vcc", "memory", "v16", "v17", "v18", "v19", "v20", "v21", "v22", "v23", "v24", "v25", "v26", "v27", "v28", "v29", "v30", "v31", "v32", "v33", "v34", "v35", "v36", "v37", "v38", "v39", "v40", "v41", "v42", "v43", "v44", "v45", "v46", "v47", "v48", "v49", "v50", "v51", "v52", "v53", "v54", "v55", "v56", "v57", "v58", "v59", "v60", "v61", "v62", "v63", "v64", "v65", "v66", "v67", "v68", "v69", "v70", "v71", "v72", "v73", "v74", "v75", "v76", "v77", "v78", "v79", "v80", "v81", "v82", "v83", "v84", "v85", "v86", "v87", "v88", "v89", "v90", "v91", "v92", "v93", "v94", "v95", "v96", "v97", "v98", "v99", "v100", "v101", "v102", "v103", "v104", "v105", "v106", "v107", "v108", "v109", "v110", "v111", "v112", "v113", "v114", "v115", "v116", "v117", "v118", "v119", "v120", "v121", "v122", "v123", "v124", "v125", "v126", "v127", "v128", "v129", "v130", "v131", "v132", "v133", "v134", "v135", "v136", "v137", "v138", "v139", "v140", "v141", "v142", "v143", "v144", "v145", "v146", "v147", "v148", "v149", "v150", "v151", "v152", "v153", "v154", "v155", "v156", "v157", "v158", "v159", "v160", "v161", "v162", "v163", "v164", "v165", "v166", "v167", "v168", "v169", "v170", "v171", "v172", "v173", "v174", "v175", "v176", "v177", "v178", "v179", "v180", "v181", "v182", "v183", "v184", "v185", "v186", "v187", "v188", "v189", "v190", "v191", "v192", "v193", "v194", "v195", "v196", "v197", "v198", "v199", "v200", "v201", "v202", "v203", "v204", "v205", "v206", "v207", "v208", "v209", "v210", "v211", "v212", "v213", "v214", "v215", "v216", "v217", "v218", "v219", "v220", "v221", "v222", "v223", "v224", "v225", "v226", "v227", "v228", "v229", "v230", "v231", "v232", "v233", "v234", "v235", "v236", "v237", "v238", "v239", "v240", "v241", "v242", "v243", "v244", "v245", "v246", "v247", "v248", "v249", "v250", "v251", "v252", "v253", "v254", "v255");
        if (slow) attn_true_max(ws, (LAS char*)lds + wave * L_RING, grp, k0, k1);
        STAMP(1);
        __syncthreads();
        return;
    }
    __builtin_amdgcn_s_setprio(2);
    const int hw = wave - 4, hid = tid - 256;
    LAS unsigned* ctl = (LAS unsigned*)(lds + L_CTL);
    const unsigned tv = hid < NWG ? *(const unsigned*)(ws + OFF_TAB + hid * 1024 + me * 4) : 0u;
    fill_lists(lds, hid, true);
    if (hid < 2) ctl[hid] = 0;
    __syncthreads();
    const unsigned len = tv >> 16;
    const unsigned* seg = (const unsigned*)(ws + OFF_SORT) + min(hid, NWG - 1) * EPC + (tv & 0xffffu);
    if (len > SEG) __hip_atomic_fetch_max(ctl + 1, len, __ATOMIC_RELAXED, __HIP_MEMORY_SCOPE_WORKGROUP);
    const __amdgpu_buffer_rsrc_t rsu = __builtin_amdgcn_make_buffer_rsrc(ws + OFF_U, 0, NN * 128, 0x00020000);
    u32x4 sel;
#pragma unroll
    for (int k = 0; k < 4; ++k) sel[k] = (i == 2 * k ? 0x3f80u : 0u) | (i == 2 * k + 1 ? 0x3f800000u : 0u);
    f32x4 acc[5] = {};
    unsigned epoch = 0;
    const unsigned lrow = L_LIST + (hw * 10 + (i >> 3)) * LROW;
    for (unsigned base = 0;; base += SEG) {
        unsigned en[SEG], rk[SEG];
#pragma unroll
        for (int k = 0; k < SEG; k += 4) {
            u32x4 v = {};
            if (base + k < len) __builtin_memcpy(&v, seg + base + k, 16);
            en[k] = v[0]; en[k + 1] = v[1]; en[k + 2] = v[2]; en[k + 3] = v[3];
        }
#pragma unroll
        for (int k = 0; k < SEG; ++k) {
            rk[k] = 0x80000000u;
            if (base + k < len) rk[k] = atomicAdd((unsigned*)(lds + L_LIST + (en[k] >> 16)), 2u);
        }
        for (unsigned win = 0;; win += 256) {
#pragma unroll
            for (int k = 0; k < SEG; ++k)
                if (rk[k] - 8u - win < 256u) *(u16*)(lds + L_LIST + (en[k] >> 16) + rk[k] - win) = (u16)en[k];
            hbar(ctl, epoch, lane);
            STAMP(1);
            const unsigned cn = lane < NPB ? *(const unsigned*)(lds + L_LIST + lane * LROW) - 8u : 0u;
            const bool mine = (unsigned)(lane - hw * 10) < 10u;
#pragma unroll 1
            for (unsigned bi = 0; bi < 256u && __any(mine && cn > win + bi); bi += 32) {
                u32x4 v[5][4];
#pragma unroll
                for (int p = 0; p < 5; ++p) {
                    const u32x2 id = *(const u32x2*)(lds + lrow + p * 2 * LROW + 8 + bi + 8 * g);
                    v[p][0] = __builtin_amdgcn_raw_buffer_load_b128(rsu, (id[0] & 0xffffu) * 128u + (i & 7) * 16u, 0, 0);
                    v[p][1] = __builtin_amdgcn_raw_buffer_load_b128(rsu, (id[0] >> 16) * 128u + (i & 7) * 16u, 0, 0);
                    v[p][2] = __builtin_amdgcn_raw_buffer_load_b128(rsu, (id[1] & 0xffffu) * 128u + (i & 7) * 16u, 0, 0);
                    v[p][3] = __builtin_amdgcn_raw_buffer_load_b128(rsu, (id[1] >> 16) * 128u + (i & 7) * 16u, 0, 0);
                }
#pragma unroll
                for (int p = 0; p < 5; ++p)
#pragma unroll
                    for (int k = 0; k < 4; ++k) acc[p] = mfma(sel, v[p][k], acc[p]);
            }
            if (!__any(cn > win + 256u)) break;
            hbar(ctl, epoch, lane);
            fill_lists(lds, hid, false);
            hbar(ctl, epoch, lane);
        }
        if (__builtin_amdgcn_readfirstlane(ctl[1]) <= base + SEG) break;
        hbar(ctl, epoch, lane);
        fill_lists(lds, hid, true);
        hbar(ctl, epoch, lane);
    }
    if (g < 2) {
#pragma unroll
        for (int p = 0; p < 5; ++p) *(f32x4*)(lds + L_ZL + (hw * 10 + 2 * p + (i >> 3)) * ZROW + (i & 7) * 32 + g * 16) = acc[p];
    }
    STAMP(2);
    const int lr = 16 * hw + i - 8 * half;
    const bool valid = (unsigned)lr < (unsigned)NPB;
    const int node = me * NPB + (valid ? lr : 0);
    u32x4 w1[4][2]; f32x4 y[4];
    if (hw < 3) {
        u32x4 w2[4][2];
#pragma unroll
        for (int eb = 0; eb < 4; ++eb)
#pragma unroll
            for (int s = 0; s < 2; ++s) {
                w1[eb][s] = *(const u32x4*)(ws + OFF_W + (eb * 2 + s) * 1024 + lane * 16);
                w2[eb][s] = *(const u32x4*)(ws + OFF_W + (8 + eb * 2 + s) * 1024 + lane * 16);
            }
        const u32x4 x0 = *(const u32x4*)(x + node * 64 + 8 * g), x1 = *(const u32x4*)(x + node * 64 + 32 + 8 * g);
        const float di = bflo(dinv[node]);
        hbar(ctl, epoch, lane);
        f32x4 z[4];
#pragma unroll
        for (int b = 0; b < 4; ++b) z[b] = *(const f32x4*)(lds + L_ZL + (valid ? lr : 0) * ZROW + b * 64 + g * 16) * di;
        const u32x4 z0 = pk8(z[0], z[1]), z1 = pk8(z[2], z[3]);
#pragma unroll
        for (int eb = 0; eb < 4; ++eb) {
            const unsigned xa = eb < 2 ? x0[2 * (eb & 1)] : x1[2 * (eb & 1)], xb = eb < 2 ? x0[2 * (eb & 1) + 1] : x1[2 * (eb & 1) + 1];
            y[eb] = mfma(w2[eb][1], z1, mfma(w2[eb][0], z0, f32x4{bflo(xa), bfhi(xa), bflo(xb), bfhi(xb)}));
        }
    } else {
        hbar(ctl, epoch, lane);
    }
    STAMP(3);
    __syncthreads();
    STAMP(4);
    const __amdgpu_buffer_rsrc_t rsb = __builtin_amdgcn_make_buffer_rsrc(ws + OFF_BOX, 0, NWG * 3 * BOXB, 0x00020000);
#pragma unroll
    for (int f = 0; f < 3; ++f) {
        const char* p = lds + ((2 - 2 * half + f) * 4 + hw) * 1024 + lane * 16;
        const f32x4 s = (*(const f32x4*)p + *(const f32x4*)(p + L_RING)) + (*(const f32x4*)(p + 2 * L_RING) + *(const f32x4*)(p + 3 * L_RING));
        const unsigned bo = ((me ^ 1) * 3 + f) * BOXB;
        __builtin_amdgcn_raw_buffer_store_b64(u32x2{min(pk(s[0], s[1]), SENT - 1), min(pk(s[2], s[3]), SENT - 1)}, rsb,
                                              bo + (hw >> 1) * 1024 + lane * 16 + (hw & 1) * 8, 0, 16);
        if (hw == 3 && g == 3) __builtin_amdgcn_raw_buffer_store_b32(min(__float_as_uint(s[3]), SENT - 1), rsb, bo + 2048 + i * 4, 0, 16);
    }
    if (hw == 3) return;
    f32x4 z[4];
#pragma unroll
    for (int b = 0; b < 4; ++b) {
        const char* p = lds + ((2 * half + hw) * 4 + b) * 1024 + lane * 16;
        z[b] = (*(const f32x4*)p + *(const f32x4*)(p + L_RING)) + (*(const f32x4*)(p + 2 * L_RING) + *(const f32x4*)(p + 3 * L_RING));
    }
    const float lown = gsum(g == 3 ? z[3][3] : 0.f);
    const u32x4 z0 = pk8(z[0], z[1]), z1 = pk8(z[2], z[3]);
    f32x4 a[4];
#pragma unroll
    for (int eb = 0; eb < 4; ++eb) a[eb] = mfma(w1[eb][1], z1, mfma(w1[eb][0], z0, f32x4{}));
    STAMP(5);
    u32x4 m0, m1; unsigned ml;
    const unsigned bo = (me * 3 + hw) * BOXB;
#pragma unroll 1
    for (int spin = 0; spin < (1 << 22); ++spin) {
        m0 = __builtin_amdgcn_raw_buffer_load_b128(rsb, bo + lane * 16, 0, 16);
        m1 = __builtin_amdgcn_raw_buffer_load_b128(rsb, bo + 1024 + lane * 16, 0, 16);
        ml = __builtin_amdgcn_raw_buffer_load_b32(rsb, bo + 2048 + i * 4, 0, 16);
        const unsigned mx = max(max(max(max(m0[0], m0[1]), max(m0[2], m0[3])), max(max(m1[0], m1[1]), max(m1[2], m1[3]))), ml);
        if (!__any(mx == SENT)) break;
        asm volatile("" ::: "memory");
    }
    STAMP(6);
    const float rl = 1.0f / (lown + __uint_as_float(ml));
#pragma unroll
    for (int eb = 0; eb < 4; ++eb) y[eb] += mfma(w1[eb][1], m1, mfma(w1[eb][0], m0, a[eb])) * rl;
    if (valid) {
        *(u32x4*)(out + node * 64 + 8 * g) = pk8(y[0], y[1]);
        *(u32x4*)(out + node * 64 + 32 + 8 * g) = pk8(y[2], y[3]);
    }
    STAMP(7);
}

extern "C" void kernel_launch(void* const* d_in, const int* in_sizes, int n_in, void* d_out, int out_size, void* d_ws, size_t ws_size,
                              hipStream_t stream) {
    const u16* x = (const u16*)d_in[0];
    k_prep<<<NWG + 125 + 2, 320, 0, stream>>>(x, (const int*)d_in[1], (const int*)d_in[2], (const u16*)d_in[4], (const u16*)d_in[5],
                                              (const u16*)d_in[6], (const u16*)d_in[7], (const u16*)d_in[8], (char*)d_ws);
    k_main<<<NWG, 512, 0, stream>>>(x, (const u16*)d_in[3], (char*)d_ws, (u16*)d_out);
}
```

```cpp
#include <hip/hip_runtime.h>
#include <hip/hip_bf16.h>

typedef unsigned u32x4 __attribute__((ext_vector_type(4)));
typedef unsigned u32x16 __attribute__((ext_vector_type(16)));
typedef unsigned u32x8 __attribute__((ext_vector_type(8)));
typedef unsigned u32x2 __attribute__((ext_vector_type(2)));
typedef float f32x4 __attribute__((ext_vector_type(4)));
typedef float f32x2 __attribute__((ext_vector_type(2)));
typedef __bf16 bf16x8 __attribute__((ext_vector_type(8)));
typedef __bf16 bf16x2 __attribute__((ext_vector_type(2)));
typedef unsigned short u16;
#define DI __device__ __forceinline__
#define LAS __attribute__((address_space(3)))
#define DRAIN() asm volatile("s_waitcnt vmcnt(0)" ::: "memory")
#define BAR() asm volatile("s_waitcnt lgkmcnt(0)\n\ts_barrier" ::: "memory")

constexpr int NN = 10000, NE = 640000, NWG = 250, EPC = NE / NWG, NPB = 40, SPIN = 1 << 20;
constexpr unsigned OFF_E = 0, OFF_FW = 128, OFF_ALL = 4096, NREP = 32, OFF_W = 8192, OFF_PF = 32768, OFF_FU = 65536, OFF_FE = 114688,
                   OFF_U = 262144, OFF_BOX = OFF_U + NN * 128, BOXB = 2176, OFF_SLOT = OFF_BOX + NWG * 3 * BOXB, SLOTB = NWG * 64,
                   OFF_OVF = OFF_SLOT + NWG * SLOTB, OVFB = EPC * 4 + 128, WS_END = OFF_OVF + NWG * OVFB;
constexpr unsigned LROW = 264, ZROW = 272, CBIAS = 160, L_ACT = 244, L_LIST = 256, L_IMG = L_LIST + NPB * LROW + 128, L_HIST = L_IMG + 20480,
                   L_ZL = L_HIST + 1024, L_Q = L_ZL + NPB * ZROW, L_RING = 57344, RINGB = 24576, L_TOT = L_RING + 4 * RINGB;
static_assert(WS_END == 9766144 && SLOTB == 16000 && OVFB == 10368 && 3 * BOXB == (256 + 152) * 16 && 8 * (19 * 64 + 32) + 16 == NN, "workspace map");
static_assert(L_IMG == 10944 && L_HIST == 31424 && L_ZL == 32448 && L_Q == 43328 && L_Q + 10240 <= L_RING, "LDS map");
constexpr unsigned long long XMAP = 0x7b5a3f1d69482e0cull;

DI float bflo(unsigned w) { return __uint_as_float(w << 16); }
DI unsigned pk(float lo, float hi) { f32x2 v = {lo, hi}; return __builtin_bit_cast(unsigned, __builtin_convertvector(v, bf16x2)); }
DI u32x4 pk8(const f32x4& a, const f32x4& b) { return u32x4{pk(a[0], a[1]), pk(a[2], a[3]), pk(b[0], b[1]), pk(b[2], b[3])}; }
DI f32x4 mfma(const u32x4& a, const u32x4& b, const f32x4& c) {
    return __builtin_amdgcn_mfma_f32_16x16x32_bf16(__builtin_bit_cast(bf16x8, a), __builtin_bit_cast(bf16x8, b), c, 0, 0, 0);
}
DI int perm(int b, int i) { return 32 * (b >> 1) + 8 * (i >> 2) + 4 * (b & 1) + (i & 3); }
DI float gsum(float v) {
    auto a = __builtin_amdgcn_permlane16_swap(__float_as_uint(v), __float_as_uint(v), false, false);
    v = __uint_as_float(a[0]) + __uint_as_float(a[1]);
    auto b = __builtin_amdgcn_permlane32_swap(__float_as_uint(v), __float_as_uint(v), false, false);
    return __uint_as_float(b[0]) + __uint_as_float(b[1]);
}
DI float gmax(float v) {
    auto a = __builtin_amdgcn_permlane16_swap(__float_as_uint(v), __float_as_uint(v), false, false);
    v = fmaxf(__uint_as_float(a[0]), __uint_as_float(a[1]));
    auto b = __builtin_amdgcn_permlane32_swap(__float_as_uint(v), __float_as_uint(v), false, false);
    return fmaxf(__uint_as_float(b[0]), __uint_as_float(b[1]));
}
DI void reflect(f32x4 (&t)[4], int g) {
    float s = 0.f;
#pragma unroll
    for (int b = 0; b < 4; ++b) s += (t[b][0] + t[b][1]) + (t[b][2] + t[b][3]);
    s = gsum(s * 0.125f - (g == 3 ? t[3][3] : 0.f)) * (1.0f / 7.0f);
#pragma unroll
    for (int b = 0; b < 4; ++b)
#pragma unroll
        for (int r = 0; r < 4; ++r) t[b][r] -= s;
    if (g == 3) t[3][3] = 0.f;
}
typedef __amdgpu_buffer_rsrc_t rsrc_t;
DI u32x4 ld128(rsrc_t r, unsigned o) { return __builtin_amdgcn_raw_buffer_load_b128(r, o, 0, 16); }
DI u32x2 ld64(rsrc_t r, unsigned o) { return __builtin_amdgcn_raw_buffer_load_b64(r, o, 0, 16); }
DI unsigned ld32(rsrc_t r, unsigned o) { return __builtin_amdgcn_raw_buffer_load_b32(r, o, 0, 16); }
DI void st128(rsrc_t r, unsigned o, const u32x4& v) { __builtin_amdgcn_raw_buffer_store_b128(v, r, o, 0, 16); }
DI void st64(rsrc_t r, unsigned o, const u32x2& v) { __builtin_amdgcn_raw_buffer_store_b64(v, r, o, 0, 16); }
DI u32x2 make_nonce(const u32x2& e) {
    u32x2 n = {(unsigned)__builtin_amdgcn_readfirstlane(e[0]) + 1u, (unsigned)__builtin_amdgcn_readfirstlane(e[1])};
    asm volatile("" : "+s"(n[0]), "+s"(n[1]));
    return n;
}
DI int lane_id() {
    int l;
    asm volatile("v_mbcnt_lo_u32_b32 %0, -1, 0\n\tv_mbcnt_hi_u32_b32 %0, -1, %0" : "=v"(l));
    __builtin_assume(l >= 0 && l < 64);
    return l;
}
template <int SLEEP> DI void wait_flag(rsrc_t r, unsigned o, const u32x2& n) {
#pragma unroll 1
    for (int s = 0; s < SPIN; ++s) {
        const u32x2 f = ld64(r, o);
        if (__all(f[0] == n[0] && f[1] == n[1])) break;
        asm volatile("s_sleep %0" :: "n"(SLEEP) : "memory");
    }
    asm volatile("" ::: "memory");
}

template <int SLEEP> DI void rbar(char* lds, unsigned word, unsigned epoch, int lane) {
    LAS unsigned* ctr = (LAS unsigned*)(lds + word);
    asm volatile("s_waitcnt lgkmcnt(0)" ::: "memory");
    if (lane == 0) __hip_atomic_fetch_add(ctr, 1u, __ATOMIC_RELAXED, __HIP_MEMORY_SCOPE_WORKGROUP);
    while (__builtin_amdgcn_readfirstlane(__hip_atomic_load(ctr, __ATOMIC_RELAXED, __HIP_MEMORY_SCOPE_WORKGROUP)) < epoch) __builtin_amdgcn_s_sleep(SLEEP);
    asm volatile("" ::: "memory");
}

DI void attn_true_max(const char* ws, rsrc_t rs, const u32x2& nonce, char* lds, unsigned red, int k0, int k1) {
    const int lane = lane_id(), i = lane & 15, g = lane >> 4;
    wait_flag<16>(rs, OFF_ALL, nonce);
    __builtin_amdgcn_fence(__ATOMIC_ACQUIRE, "agent");
    const u16* U = (const u16*)(ws + OFF_U);
#pragma unroll 1
    for (int qb = 0; qb < 5; ++qb) {
        const u32x4 q0 = *(const u32x4*)(lds + L_Q + qb * 2048 + lane * 16);
        u32x4 q1 = *(const u32x4*)(lds + L_Q + qb * 2048 + 1024 + lane * 16);
        if (g == 3) q1[3] &= 0xffffu;
        float m = -INFINITY;
#pragma unroll 1
        for (int kb = 0; kb < NN; kb += 16) {
            const f32x4 s = mfma(*(const u32x4*)(U + (kb + i) * 64 + 32 + 8 * g), q1, mfma(*(const u32x4*)(U + (kb + i) * 64 + 8 * g), q0, f32x4{}));
            m = fmaxf(fmaxf(m, fmaxf(s[0], s[1])), fmaxf(s[2], s[3]));
        }
        m = gmax(m);
        f32x4 o[4] = {};
#pragma unroll 1
        for (int kb = k0; kb < k1; kb += 32) {
            const bool two = kb + 16 < k1;
            const int kc = two ? kb + 16 : kb;
            f32x4 pa = mfma(*(const u32x4*)(U + (kb + i) * 64 + 32 + 8 * g), q1, mfma(*(const u32x4*)(U + (kb + i) * 64 + 8 * g), q0, f32x4{}));
            f32x4 pb = mfma(*(const u32x4*)(U + (kc + i) * 64 + 32 + 8 * g), q1, mfma(*(const u32x4*)(U + (kc + i) * 64 + 8 * g), q0, f32x4{}));
#pragma unroll
            for (int r = 0; r < 4; ++r) { pa[r] = __builtin_amdgcn_exp2f(pa[r] - m); pb[r] = two ? __builtin_amdgcn_exp2f(pb[r] - m) : 0.f; }
            const u32x4 p = pk8(pa, pb);
#pragma unroll
            for (int db = 0; db < 4; ++db) {
                const u16* va = U + (kb + 4 * g) * 64 + 16 * db + i; const u16* vb = U + (kc + 4 * g) * 64 + 16 * db + i;
                const u32x4 vf = {(unsigned)va[0] | ((unsigned)va[64] << 16), (unsigned)va[128] | ((unsigned)va[192] << 16),
                                  (unsigned)vb[0] | ((unsigned)vb[64] << 16), (unsigned)vb[128] | ((unsigned)vb[192] << 16)};
                o[db] = mfma(vf, p, o[db]);
            }
        }
#pragma unroll
        for (int db = 0; db < 4; ++db) *(f32x4*)(lds + red + ((qb + 4) % 6 * 4 + db) * 1024 + lane * 16) = o[db];
    }
}

__global__ __launch_bounds__(512, 2) void k_gps(char* ws, const u16* __restrict__ x, const u16* __restrict__ wq, const u16* __restrict__ wk,
                                                const int* __restrict__ dst, const int* __restrict__ src, const u16* __restrict__ dinv,
                                                u16* __restrict__ out, const u16* __restrict__ wm, const u16* __restrict__ wv,
                                                const u16* __restrict__ wo) {
    __shared__ __attribute__((aligned(16))) char lds[L_TOT];
    const int bid = blockIdx.x;
    const int wave = __builtin_amdgcn_readfirstlane(threadIdx.x) >> 6;
    const int me = (bid & ~15) | ((XMAP >> (4 * (bid & 15))) & 15);
    u32x2 anonce;
    u32x16 ed; u32x8 ee;
    asm volatile(
            "s_load_dwordx8 s[48:55], %[karg], 0\n"
            "s_load_dwordx4 s[68:71], %[karg], 32\n"
            "s_cmp_gt_u32 %[bid], 7\n"
            "s_cbranch_scc1 30f\n"
            "v_mul_u32_u24 v1, 52, v0\n"
            "s_getpc_b64 vcc\n"
            "v_min_u32 v1, 24576, v1\n"
            "s_movk_i32 m0, 32448\n"
            "s_and_b32 vcc_lo, vcc_lo, 0xffffff00\n"
            "global_load_lds_dword v1, vcc\n"
            "30:\n"
            "s_lshr_b32 s64, %[me], 1\n"
            "s_cmp_gt_u32 s64, 124\n"
            "s_cbranch_scc1 99f\n"
            "s_and_b32 s63, %[me], 1\n"
            "s_cmp_gt_u32 %[wave], 3\n"
            "s_cbranch_scc1 31f\n"
            "s_setprio 3\n"
            "v_bfe_u32 v1, v0, 3, 3\n"
            "s_mul_i32 s65, s64, 5\n"
            "v_and_b32 v2, 6, v1\n"
            "s_sub_u32 s56, 4, %[wave]\n"
            "v_xor_b32 v2, v0, v2\n"
            "s_cmp_lg_u32 s63, 0\n"
            "v_and_b32 v2, 7, v2\n"
            "s_cselect_b32 s62, s56, %[wave]\n"
            "v_lshl_or_b32 v2, v1, 3, v2\n"
            "s_add_u32 s65, s65, s62\n"
            "v_lshlrev_b32 v12, 4, v2\n"
            "s_mul_i32 s57, %[wave], 24576\n"
            "v_lshl_or_b32 v2, s65, 11, v12\n"
            "s_add_u32 s57, s57, 57344\n"
            "v_and_b32 v1, 4, v1\n"
            "s_add_u32 m0, s57, 4096\n"
            "v_lshl_add_u32 v1, v1, 7, v12\n"
            "s_and_b32 s62, %[wave], 1\n"
            "v_lshl_or_b32 v1, s62, 12, v1\n"
            "s_cmp_lt_u32 %[wave], 2\n"
            "s_waitcnt lgkmcnt(0)\n"
            "global_load_lds_dwordx4 v2, s[50:51]\n"
            "global_load_lds_dwordx4 v2, s[50:51] offset:1024\n"
            "s_cselect_b64 s[52:53], s[52:53], s[54:55]\n"
            "s_mov_b32 s40, s48\n"
            "s_and_b32 s41, s49, 0xffff\n"
            "s_mov_b32 s42, 9766144\n"
            "s_mov_b32 s43, 0x20000\n"
            "s_lshl2_add_u32 s62, s63, %[wave]\n"
            "s_mov_b32 m0, s57\n"
            "buffer_load_dwordx2 v[162:163], off, s[40:43], 0 sc1\n"
            "s_add_u32 s59, s62, 304\n"
            "v_lshl_or_b32 v2, s59, 12, v12\n"
            "s_cmp_lg_u32 %[wave], 3\n"
            "global_load_lds_dwordx4 v2, s[50:51] offset:0\n"
            "global_load_lds_dwordx4 v2, s[50:51] offset:1024\n"
            "global_load_lds_dwordx4 v2, s[50:51] offset:2048\n"
            "global_load_lds_dwordx4 v2, s[50:51] offset:3072\n"
            "s_cbranch_scc1 32f\n"
            "s_lshl_b32 s59, s63, 1\n"
            "s_sub_u32 s59, s65, s59\n"
            "s_add_u32 s59, s59, 1\n"
            "s_add_u32 m0, s57, 6144\n"
            "v_lshl_or_b32 v2, s59, 11, v12\n"
            "global_load_lds_dwordx4 v2, s[50:51]\n"
            "global_load_lds_dwordx4 v2, s[50:51] offset:1024\n"
            "32:\n"
            "s_add_u32 m0, s57, 16384\n"
            "v_and_b32 v2, 15, v0\n"
            "global_load_lds_dwordx4 v1, s[52:53] offset:0\n"
            "s_add_u32 m0, s57, 15360\n"
            "v_bfe_u32 v3, v0, 4, 2\n"
            "global_load_lds_dwordx4 v1, s[52:53] offset:2048\n"
            "s_add_u32 m0, s57, 17920\n"
            "s_nop 0\n"
            "global_load_lds_dwordx4 v1, s[52:53] offset:512\n"
            "s_add_u32 m0, s57, 16896\n"
            "s_nop 0\n"
            "global_load_lds_dwordx4 v1, s[52:53] offset:2560\n"
            "v_and_b32 v1, 6, v2\n"
            "v_xor_b32 v1, v3, v1\n"
            "v_lshlrev_b32 v11, 7, v2\n"
            "v_lshl_or_b32 v1, v1, 4, v11\n"
            "v_add_u32 v16, s57, v1\n"
            "v_add_u32 v10, 122880, v1\n"
            "v_xor_b32 v187, 64, v10\n"
            "v_xor_b32 v17, 64, v16\n"
            "v_lshl_or_b32 v11, v3, 4, v11\n"
            "v_and_b32 v14, 3, v2\n"
            "v_lshrrev_b32 v13, 2, v2\n"
            "v_cmp_eq_u32 vcc, 0, v14\n"
            "v_mov_b32 v1, 0x3f803f80\n"
            "v_mov_b32 v182, 1.0\n"
            "v_cndmask_b32 v176, 0, v1, vcc\n"
            "v_cmp_ne_u32 vcc, 3, v3\n"
            "v_mov_b32 v1, 0xffff\n"
            "v_mov_b32 v177, v176\n"
            "v_cndmask_b32_e64 v8, v1, -1, vcc\n"
            "v_cndmask_b32_e64 v183, 1.0, 0, vcc\n"
            "v_cmp_eq_u32 vcc, 1, v14\n"
            "v_mov_b64 v[178:179], v[176:177]\n"
            "v_mov_b32 v180, v176\n"
            "v_cndmask_b32 v181, v176, v183, vcc\n"
            "v_mul_f32 v184, 0xc1000000, v183\n"
            "v_mov_b32 v185, 0x3c800000\n"
            "s_lshl_b32 s58, s65, 11\n"
            "s_add_u32 s58, s58, 262144\n"
            "s_lshl_b32 s59, s65, 6\n"
            "s_add_u32 s59, s59, 65536\n"
            "s_add_u32 s61, s63, %[wave]\n"
            "s_cmp_lt_u32 s61, 3\n"
            "s_cselect_b32 s58, s58, 9766144\n"
            "v_lshl_or_b32 v1, v3, 2, v13\n"
            "v_and_b32 v4, 6, v1\n"
            "v_lshlrev_b32 v1, 7, v1\n"
            "v_xor_b32 v9, v14, v4\n"
            "v_lshl_or_b32 v9, v9, 4, v1\n"
            "v_add_u32 v9, 73728, v9\n"
            "v_xor_b32 v186, 64, v9\n"
            "v_lshl_or_b32 v1, v4, 4, v1\n"
            "v_lshl_or_b32 v1, v14, 3, v1\n"
            "v_add_u32 v20, s57, v1\n"
            "v_xor_b32 v21, 32, v20\n"
            "v_xor_b32 v22, 64, v20\n"
            "v_xor_b32 v23, 96, v20\n"
            "v_and_b32 v4, 63, v0\n"
            "v_lshlrev_b32 v5, 4, v4\n"
            "v_add_u32 v19, s57, v5\n"
            "s_mul_i32 s61, s62, 155648\n"
            "s_add_u32 s61, s61, 262144\n"
            "s_mov_b64 s[44:45], s[40:41]\n"
            "s_mov_b32 s46, s42\n"
            "s_mov_b32 s47, s43\n"
            "s_sub_u32 s61, s61, s57\n"
            "v_add_u32 v18, s61, v12\n"
            "s_mul_i32 s61, s62, 4864\n"
            "s_add_u32 s61, s61, 65536\n"
            "v_lshl_add_u32 v4, v4, 6, s61\n"
            "s_lshl_b32 s61, s56, 11\n"
            "v_add_u32 v188, s61, v5\n"
            "v_mov_b32 v160, 0\n"
            "v_mov_b32 v161, 1\n"
            "s_mov_b32 s82, 0x3e38aa3b\n"
            "s_waitcnt vmcnt(9)\n"
            "ds_read_b128 v[208:211], v16 offset:4096\n"
            "ds_read_b128 v[212:215], v17 offset:4096\n"
            "s_waitcnt lgkmcnt(0)\n"
            "v_dot2_f32_bf16 v48, v208, v208, 0\n"
            "v_dot2_f32_bf16 v48, v209, v209, v48\n"
            "v_dot2_f32_bf16 v48, v210, v210, v48\n"
            "v_dot2_f32_bf16 v48, v211, v211, v48\n"
            "v_dot2_f32_bf16 v48, v212, v212, v48\n"
            "v_dot2_f32_bf16 v48, v213, v213, v48\n"
            "v_dot2_f32_bf16 v48, v214, v214, v48\n"
            "v_dot2_f32_bf16 v48, v215, v215, v48\n"
            "v_mfma_f32_16x16x32_bf16 v[40:43], v[176:179], v[208:211], 0\n"
            "v_lshlrev_b32 v24, 16, v208\n"
            "v_and_b32 v25, 0xffff0000, v208\n"
            "v_mfma_f32_16x16x4_f32 v[44:47], v182, v48, 0\n"
            "v_lshlrev_b32 v26, 16, v209\n"
            "v_and_b32 v27, 0xffff0000, v209\n"
            "v_lshlrev_b32 v28, 16, v210\n"
            "v_and_b32 v29, 0xffff0000, v210\n"
            "v_lshlrev_b32 v30, 16, v211\n"
            "v_and_b32 v31, 0xffff0000, v211\n"
            "v_mfma_f32_16x16x32_bf16 v[40:43], v[178:181], v[212:215], v[40:43]\n"
            "v_lshlrev_b32 v38, 16, v215\n"
            "v_and_b32 v39, 0xffff0000, v215\n"
            "v_lshlrev_b32 v32, 16, v212\n"
            "v_and_b32 v33, 0xffff0000, v212\n"
            "v_lshlrev_b32 v34, 16, v213\n"
            "v_and_b32 v35, 0xffff0000, v213\n"
            "v_lshlrev_b32 v36, 16, v214\n"
            "v_and_b32 v37, 0xffff0000, v214\n"
            "v_mul_f32 v42, v185, v40\n"
            "v_fma_f32 v44, -v42, v40, v44\n"
            "v_mul_f32_e64 v44, |v44|, v185\n"
            "v_cvt_pk_bf16_f32 v44, 0, v44\n"
            "v_add_f32 v44, 0x3727c5ac, v44\n"
            "v_cvt_pk_bf16_f32 v44, 0, v44\n"
            "v_sqrt_f32 v44, v44\n"
            "v_mul_f32 v41, 0x3e124925, v41\n"
            "v_cvt_pk_bf16_f32 v44, 0, v44\n"
            "v_rcp_f32 v50, v44\n"
            "v_fmac_f32 v41, 0xbc924925, v40\n"
            "v_mul_f32 v51, v41, v50\n"
            "v_pk_fma_f32 v[224:225], v[24:25], v[50:51], v[50:51] op_sel:[0,0,1] op_sel_hi:[1,0,1]\n"
            "v_pk_fma_f32 v[226:227], v[26:27], v[50:51], v[50:51] op_sel:[0,0,1] op_sel_hi:[1,0,1]\n"
            "v_pk_fma_f32 v[228:229], v[28:29], v[50:51], v[50:51] op_sel:[0,0,1] op_sel_hi:[1,0,1]\n"
            "v_pk_fma_f32 v[230:231], v[30:31], v[50:51], v[50:51] op_sel:[0,0,1] op_sel_hi:[1,0,1]\n"
            "v_pk_fma_f32 v[238:239], v[38:39], v[50:51], v[50:51] op_sel:[0,0,1] op_sel_hi:[1,0,1]\n"
            "v_cvt_pk_bf16_f32 v208, v224, v225\n"
            "v_cvt_pk_bf16_f32 v209, v226, v227\n"
            "v_cvt_pk_bf16_f32 v210, v228, v229\n"
            "v_cvt_pk_bf16_f32 v211, v230, v231\n"
            "v_pk_fma_f32 v[232:233], v[32:33], v[50:51], v[50:51] op_sel:[0,0,1] op_sel_hi:[1,0,1]\n"
            "v_pk_fma_f32 v[234:235], v[34:35], v[50:51], v[50:51] op_sel:[0,0,1] op_sel_hi:[1,0,1]\n"
            "v_pk_fma_f32 v[236:237], v[36:37], v[50:51], v[50:51] op_sel:[0,0,1] op_sel_hi:[1,0,1]\n"
            "v_cvt_pk_bf16_f32 v215, v238, v239\n"
            "v_cvt_pk_bf16_f32 v212, v232, v233\n"
            "v_cvt_pk_bf16_f32 v213, v234, v235\n"
            "v_cvt_pk_bf16_f32 v214, v236, v237\n"
            "v_and_or_b32 v215, v215, v8, v183\n"
            "buffer_store_dwordx4 v[208:211], v11, s[40:43], s58 offen sc1\n"
            "buffer_store_dwordx4 v[212:215], v11, s[40:43], s58 offen offset:64 sc1\n"
            "s_waitcnt vmcnt(6)\n"
            "ds_read_b128 v[192:195], v16 offset:0\n"
            "ds_read_b128 v[196:199], v17 offset:0\n"
            "ds_read_b128 v[200:203], v16 offset:2048\n"
            "ds_read_b128 v[204:207], v17 offset:2048\n"
            "ds_read_b128 v[216:219], v16 offset:6144\n"
            "ds_read_b128 v[220:223], v17 offset:6144\n"
            "v_readfirstlane_b32 s80, v162\n"
            "v_readfirstlane_b32 s81, v163\n"
            "v_mul_f32_e64 v46, -v42, v50\n"
            "v_pk_fma_f32 v[24:25], v[24:25], v[50:51], v[46:47] op_sel_hi:[1,0,0]\n"
            "v_pk_fma_f32 v[26:27], v[26:27], v[50:51], v[46:47] op_sel_hi:[1,0,0]\n"
            "v_pk_fma_f32 v[28:29], v[28:29], v[50:51], v[46:47] op_sel_hi:[1,0,0]\n"
            "v_pk_fma_f32 v[30:31], v[30:31], v[50:51], v[46:47] op_sel_hi:[1,0,0]\n"
            "v_pk_fma_f32 v[32:33], v[32:33], v[50:51], v[46:47] op_sel_hi:[1,0,0]\n"
            "v_pk_fma_f32 v[34:35], v[34:35], v[50:51], v[46:47] op_sel_hi:[1,0,0]\n"
            "v_pk_fma_f32 v[36:37], v[36:37], v[50:51], v[46:47] op_sel_hi:[1,0,0]\n"
            "v_pk_fma_f32 v[38:39], v[38:39], v[50:51], v[46:47] op_sel_hi:[1,0,0]\n"
            "v_cvt_pk_bf16_f32 v144, v24, v25\n"
            "v_cvt_pk_bf16_f32 v145, v26, v27\n"
            "v_cvt_pk_bf16_f32 v146, v28, v29\n"
            "v_cvt_pk_bf16_f32 v147, v30, v31\n"
            "v_cvt_pk_bf16_f32 v148, v32, v33\n"
            "v_cvt_pk_bf16_f32 v149, v34, v35\n"
            "v_cvt_pk_bf16_f32 v150, v36, v37\n"
            "v_cvt_pk_bf16_f32 v151, v38, v39\n"
            "s_nop 1\n"
            "s_add_u32 s80, s80, 1\n"
            "s_waitcnt lgkmcnt(0)\n"
            "s_waitcnt vmcnt(2)\n"
            "s_barrier\n"
            "ds_read_b64_tr_b16 v[64:65], v9 offset:0\n"
            "ds_read_b64_tr_b16 v[66:67], v9 offset:2048\n"
            "ds_read_b64_tr_b16 v[68:69], v9 offset:24576\n"
            "ds_read_b64_tr_b16 v[70:71], v9 offset:26624\n"
            "ds_read_b64_tr_b16 v[72:73], v9 offset:8\n"
            "ds_read_b64_tr_b16 v[74:75], v9 offset:2056\n"
            "ds_read_b64_tr_b16 v[76:77], v9 offset:24584\n"
            "ds_read_b64_tr_b16 v[78:79], v9 offset:26632\n"
            "ds_read_b64_tr_b16 v[80:81], v186 offset:0\n"
            "ds_read_b64_tr_b16 v[82:83], v186 offset:2048\n"
            "ds_read_b64_tr_b16 v[84:85], v186 offset:24576\n"
            "ds_read_b64_tr_b16 v[86:87], v186 offset:26624\n"
            "ds_read_b64_tr_b16 v[88:89], v186 offset:8\n"
            "ds_read_b64_tr_b16 v[90:91], v186 offset:2056\n"
            "ds_read_b64_tr_b16 v[92:93], v186 offset:24584\n"
            "ds_read_b64_tr_b16 v[94:95], v186 offset:26632\n"
            "ds_read_b128 v[96:99], v10 offset:0\n"
            "ds_read_b128 v[100:103], v187 offset:0\n"
            "ds_read_b128 v[104:107], v10 offset:2048\n"
            "ds_read_b128 v[108:111], v187 offset:2048\n"
            "ds_read_b128 v[112:115], v10 offset:24576\n"
            "ds_read_b128 v[116:119], v187 offset:24576\n"
            "ds_read_b128 v[120:123], v10 offset:26624\n"
            "ds_read_b128 v[124:127], v187 offset:26624\n"
            "s_cmp_eq_u32 s58, s42\n"
            "s_cbranch_scc1 20f\n"
            "v_mov_b64 v[162:163], s[80:81]\n"
            "s_mov_b64 exec, 1\n"
            "s_waitcnt vmcnt(0)\n"
            "buffer_store_dwordx2 v[162:163], off, s[40:43], s59 sc1\n"
            "s_mov_b64 exec, -1\n"
            "20:\n"
            "s_cmp_eq_u32 %[wave], 3\n"
            "s_cselect_b32 s60, 1, 0\n"
            "s_cbranch_scc1 25f\n"
            "v_dot2_f32_bf16 v48, v192, v192, 0\n"
            "v_dot2_f32_bf16 v48, v193, v193, v48\n"
            "v_dot2_f32_bf16 v48, v194, v194, v48\n"
            "v_dot2_f32_bf16 v48, v195, v195, v48\n"
            "v_dot2_f32_bf16 v48, v196, v196, v48\n"
            "v_dot2_f32_bf16 v48, v197, v197, v48\n"
            "v_dot2_f32_bf16 v48, v198, v198, v48\n"
            "v_dot2_f32_bf16 v48, v199, v199, v48\n"
            "v_mfma_f32_16x16x32_bf16 v[40:43], v[176:179], v[192:195], 0\n"
            "v_lshlrev_b32 v38, 16, v199\n"
            "v_and_b32 v39, 0xffff0000, v199\n"
            "v_mfma_f32_16x16x4_f32 v[44:47], v182, v48, 0\n"
            "v_lshlrev_b32 v24, 16, v192\n"
            "v_and_b32 v25, 0xffff0000, v192\n"
            "v_lshlrev_b32 v26, 16, v193\n"
            "v_and_b32 v27, 0xffff0000, v193\n"
            "v_lshlrev_b32 v28, 16, v194\n"
            "v_and_b32 v29, 0xffff0000, v194\n"
            "v_mfma_f32_16x16x32_bf16 v[40:43], v[178:181], v[196:199], v[40:43]\n"
            "v_lshlrev_b32 v30, 16, v195\n"
            "v_and_b32 v31, 0xffff0000, v195\n"
            "v_lshlrev_b32 v32, 16, v196\n"
            "v_and_b32 v33, 0xffff0000, v196\n"
            "v_lshlrev_b32 v34, 16, v197\n"
            "v_and_b32 v35, 0xffff0000, v197\n"
            "v_lshlrev_b32 v36, 16, v198\n"
            "v_and_b32 v37, 0xffff0000, v198\n"
            "v_mul_f32 v42, v185, v40\n"
            "v_fma_f32 v44, -v42, v40, v44\n"
            "v_mul_f32_e64 v44, |v44|, v185\n"
            "v_cvt_pk_bf16_f32 v44, 0, v44\n"
            "v_add_f32 v44, 0x3727c5ac, v44\n"
            "v_cvt_pk_bf16_f32 v44, 0, v44\n"
            "v_sqrt_f32 v44, v44\n"
            "v_mul_f32 v41, 0x3e124925, v41\n"
            "v_cvt_pk_bf16_f32 v44, 0, v44\n"
            "v_rcp_f32 v50, v44\n"
            "v_fmac_f32 v41, 0xbc924925, v40\n"
            "v_mul_f32 v51, v41, v50\n"
            "v_pk_fma_f32 v[38:39], v[38:39], v[50:51], v[50:51] op_sel:[0,0,1] op_sel_hi:[1,0,1]\n"
            "v_pk_fma_f32 v[24:25], v[24:25], v[50:51], v[50:51] op_sel:[0,0,1] op_sel_hi:[1,0,1]\n"
            "v_pk_fma_f32 v[26:27], v[26:27], v[50:51], v[50:51] op_sel:[0,0,1] op_sel_hi:[1,0,1]\n"
            "v_pk_fma_f32 v[28:29], v[28:29], v[50:51], v[50:51] op_sel:[0,0,1] op_sel_hi:[1,0,1]\n"
            "v_pk_fma_f32 v[30:31], v[30:31], v[50:51], v[50:51] op_sel:[0,0,1] op_sel_hi:[1,0,1]\n"
            "v_pk_fma_f32 v[32:33], v[32:33], v[50:51], v[50:51] op_sel:[0,0,1] op_sel_hi:[1,0,1]\n"
            "v_pk_fma_f32 v[34:35], v[34:35], v[50:51], v[50:51] op_sel:[0,0,1] op_sel_hi:[1,0,1]\n"
            "v_pk_fma_f32 v[36:37], v[36:37], v[50:51], v[50:51] op_sel:[0,0,1] op_sel_hi:[1,0,1]\n"
            "v_cvt_pk_bf16_f32 v199, v38, v39\n"
            "v_cvt_pk_bf16_f32 v192, v24, v25\n"
            "v_cvt_pk_bf16_f32 v193, v26, v27\n"
            "v_cvt_pk_bf16_f32 v194, v28, v29\n"
            "v_cvt_pk_bf16_f32 v195, v30, v31\n"
            "v_cvt_pk_bf16_f32 v196, v32, v33\n"
            "v_cvt_pk_bf16_f32 v197, v34, v35\n"
            "v_cvt_pk_bf16_f32 v198, v36, v37\n"
            "v_and_or_b32 v199, v199, v8, v183\n"
            "s_nop 1\n"
            "s_branch 26f\n"
            "25:\n"
            "v_dot2_f32_bf16 v48, v216, v216, 0\n"
            "v_dot2_f32_bf16 v48, v217, v217, v48\n"
            "v_dot2_f32_bf16 v48, v218, v218, v48\n"
            "v_dot2_f32_bf16 v48, v219, v219, v48\n"
            "v_dot2_f32_bf16 v48, v220, v220, v48\n"
            "v_dot2_f32_bf16 v48, v221, v221, v48\n"
            "v_dot2_f32_bf16 v48, v222, v222, v48\n"
            "v_dot2_f32_bf16 v48, v223, v223, v48\n"
            "v_mfma_f32_16x16x32_bf16 v[40:43], v[176:179], v[216:219], 0\n"
            "v_lshlrev_b32 v24, 16, v216\n"
            "v_and_b32 v25, 0xffff0000, v216\n"
            "v_mfma_f32_16x16x4_f32 v[44:47], v182, v48, 0\n"
            "v_lshlrev_b32 v26, 16, v217\n"
            "v_and_b32 v27, 0xffff0000, v217\n"
            "v_lshlrev_b32 v28, 16, v218\n"
            "v_and_b32 v29, 0xffff0000, v218\n"
            "v_lshlrev_b32 v30, 16, v219\n"
            "v_and_b32 v31, 0xffff0000, v219\n"
            "v_mfma_f32_16x16x32_bf16 v[40:43], v[178:181], v[220:223], v[40:43]\n"
            "v_lshlrev_b32 v32, 16, v220\n"
            "v_and_b32 v33, 0xffff0000, v220\n"
            "v_lshlrev_b32 v34, 16, v221\n"
            "v_and_b32 v35, 0xffff0000, v221\n"
            "v_lshlrev_b32 v36, 16, v222\n"
            "v_and_b32 v37, 0xffff0000, v222\n"
            "v_lshlrev_b32 v38, 16, v223\n"
            "v_and_b32 v39, 0xffff0000, v223\n"
            "v_mul_f32 v42, v185, v40\n"
            "v_fma_f32 v44, -v42, v40, v44\n"
            "v_mul_f32_e64 v44, |v44|, v185\n"
            "v_cvt_pk_bf16_f32 v44, 0, v44\n"
            "v_add_f32 v44, 0x3727c5ac, v44\n"
            "v_cvt_pk_bf16_f32 v44, 0, v44\n"
            "v_sqrt_f32 v44, v44\n"
            "s_nop 0\n"
            "v_cvt_pk_bf16_f32 v44, 0, v44\n"
            "v_rcp_f32 v50, v44\n"
            "s_nop 0\n"
            "v_mul_f32_e64 v46, -v42, v50\n"
            "v_pk_fma_f32 v[24:25], v[24:25], v[50:51], v[46:47] op_sel_hi:[1,0,0]\n"
            "v_pk_fma_f32 v[26:27], v[26:27], v[50:51], v[46:47] op_sel_hi:[1,0,0]\n"
            "v_pk_fma_f32 v[28:29], v[28:29], v[50:51], v[46:47] op_sel_hi:[1,0,0]\n"
            "v_pk_fma_f32 v[30:31], v[30:31], v[50:51], v[46:47] op_sel_hi:[1,0,0]\n"
            "v_pk_fma_f32 v[32:33], v[32:33], v[50:51], v[46:47] op_sel_hi:[1,0,0]\n"
            "v_pk_fma_f32 v[34:35], v[34:35], v[50:51], v[46:47] op_sel_hi:[1,0,0]\n"
            "v_pk_fma_f32 v[36:37], v[36:37], v[50:51], v[46:47] op_sel_hi:[1,0,0]\n"
            "v_pk_fma_f32 v[38:39], v[38:39], v[50:51], v[46:47] op_sel_hi:[1,0,0]\n"
            "v_cvt_pk_bf16_f32 v152, v24, v25\n"
            "v_cvt_pk_bf16_f32 v153, v26, v27\n"
            "v_cvt_pk_bf16_f32 v154, v28, v29\n"
            "v_cvt_pk_bf16_f32 v155, v30, v31\n"
            "v_cvt_pk_bf16_f32 v156, v32, v33\n"
            "v_cvt_pk_bf16_f32 v157, v34, v35\n"
            "v_cvt_pk_bf16_f32 v158, v36, v37\n"
            "v_cvt_pk_bf16_f32 v159, v38, v39\n"
            "s_nop 1\n"
            "26:\n"
            "s_cmp_eq_u32 0, 0\n"
            "s_waitcnt lgkmcnt(0)\n"
            "23:\n"
            "v_mfma_f32_16x16x32_bf16 v[128:131], v[64:67], v[144:147], 0\n"
            "v_dot2_f32_bf16 v248, v200, v200, 0\n"
            "v_dot2_f32_bf16 v248, v201, v201, v248\n"
            "v_mfma_f32_16x16x32_bf16 v[132:135], v[72:75], v[144:147], 0\n"
            "v_dot2_f32_bf16 v248, v202, v202, v248\n"
            "v_dot2_f32_bf16 v248, v203, v203, v248\n"
            "v_mfma_f32_16x16x32_bf16 v[128:131], v[68:71], v[148:151], v[128:131]\n"
            "v_dot2_f32_bf16 v248, v204, v204, v248\n"
            "v_dot2_f32_bf16 v248, v205, v205, v248\n"
            "v_mfma_f32_16x16x32_bf16 v[132:135], v[76:79], v[148:151], v[132:135]\n"
            "v_dot2_f32_bf16 v248, v206, v206, v248\n"
            "v_dot2_f32_bf16 v248, v207, v207, v248\n"
            "v_mfma_f32_16x16x32_bf16 v[136:139], v[80:83], v[144:147], 0\n"
            "v_lshlrev_b32 v238, 16, v207\n"
            "v_and_b32 v239, 0xffff0000, v207\n"
            "v_mfma_f32_16x16x32_bf16 v[140:143], v[88:91], v[144:147], 0\n"
            "v_cvt_pk_bf16_f32 v144, v128, v129\n"
            "v_cvt_pk_bf16_f32 v145, v130, v131\n"
            "v_mfma_f32_16x16x32_bf16 v[136:139], v[84:87], v[148:151], v[136:139]\n"
            "v_cvt_pk_bf16_f32 v146, v132, v133\n"
            "v_cvt_pk_bf16_f32 v147, v134, v135\n"
            "v_mfma_f32_16x16x32_bf16 v[140:143], v[92:95], v[148:151], v[140:143]\n"
            "v_lshlrev_b32 v224, 16, v200\n"
            "v_and_b32 v225, 0xffff0000, v200\n"
            "v_mfma_f32_16x16x32_bf16 v[128:131], v[96:99], v[144:147], 0\n"
            "v_lshlrev_b32 v226, 16, v201\n"
            "v_and_b32 v227, 0xffff0000, v201\n"
            "v_cvt_pk_bf16_f32 v148, v136, v137\n"
            "v_cvt_pk_bf16_f32 v149, v138, v139\n"
            "v_mfma_f32_16x16x32_bf16 v[132:135], v[104:107], v[144:147], 0\n"
            "v_cvt_pk_bf16_f32 v150, v140, v141\n"
            "v_cvt_pk_bf16_f32 v151, v142, v143\n"
            "v_mfma_f32_16x16x32_bf16 v[136:139], v[112:115], v[144:147], 0\n"
            "v_lshlrev_b32 v228, 16, v202\n"
            "v_and_b32 v229, 0xffff0000, v202\n"
            "v_mfma_f32_16x16x32_bf16 v[140:143], v[120:123], v[144:147], 0\n"
            "v_lshlrev_b32 v230, 16, v203\n"
            "v_and_b32 v231, 0xffff0000, v203\n"
            "v_mfma_f32_16x16x32_bf16 v[128:131], v[100:103], v[148:151], v[128:131]\n"
            "v_lshlrev_b32 v232, 16, v204\n"
            "v_and_b32 v233, 0xffff0000, v204\n"
            "v_mfma_f32_16x16x32_bf16 v[132:135], v[108:111], v[148:151], v[132:135]\n"
            "v_lshlrev_b32 v234, 16, v205\n"
            "v_and_b32 v235, 0xffff0000, v205\n"
            "v_mfma_f32_16x16x32_bf16 v[136:139], v[116:119], v[148:151], v[136:139]\n"
            "v_lshlrev_b32 v236, 16, v206\n"
            "v_and_b32 v237, 0xffff0000, v206\n"
            "v_mfma_f32_16x16x32_bf16 v[140:143], v[124:127], v[148:151], v[140:143]\n"
            "v_pk_add_f32 v[52:53], v[128:129], v[130:131]\n"
            "v_mfma_f32_16x16x32_bf16 v[240:243], v[176:179], v[200:203], 0\n"
            "s_cbranch_scc1 36f\n"
            "s_mov_b64 exec, 0xfff\n"
            "buffer_load_dwordx2 v[6:7], v4, s[40:43], 0 offen sc1\n"
            "s_mov_b64 exec, -1\n"
            "36:\n"
            "v_pk_add_f32 v[54:55], v[132:133], v[134:135]\n"
            "v_pk_add_f32 v[52:53], v[52:53], v[54:55]\n"
            "v_mfma_f32_16x16x4_f32 v[244:247], v182, v248, 0\n"
            "v_pk_add_f32 v[56:57], v[136:137], v[138:139]\n"
            "v_mfma_f32_16x16x32_bf16 v[240:243], v[178:181], v[204:207], v[240:243]\n"
            "s_nop 0\n"
            "v_pk_add_f32 v[58:59], v[140:141], v[142:143]\n"
            "v_pk_add_f32 v[56:57], v[56:57], v[58:59]\n"
            "v_pk_add_f32 v[52:53], v[52:53], v[56:57]\n"
            "v_add_f32 v52, v52, v53\n"
            "v_fmac_f32 v52, v184, v143\n"
            "s_nop 1\n"
            "v_mfma_f32_16x16x4_f32 v[56:59], v182, v52, 0\n"
            "v_mul_f32 v242, v185, v240\n"
            "v_fma_f32 v244, -v242, v240, v244\n"
            "v_mul_f32_e64 v244, |v244|, v185\n"
            "v_cvt_pk_bf16_f32 v244, 0, v244\n"
            "v_add_f32 v244, 0x3727c5ac, v244\n"
            "v_cvt_pk_bf16_f32 v244, 0, v244\n"
            "v_sqrt_f32 v244, v244\n"
            "v_mul_f32 v241, 0x3e124925, v241\n"
            "v_cvt_pk_bf16_f32 v244, 0, v244\n"
            "v_rcp_f32 v250, v244\n"
            "v_mul_f32 v54, 0xbb530bb1, v56\n"
            "v_pk_fma_f32 v[128:129], v[128:129], s[82:83], v[54:55] op_sel_hi:[1,0,0]\n"
            "v_cvt_pk_bf16_f32 v144, v128, v129\n"
            "v_pk_fma_f32 v[130:131], v[130:131], s[82:83], v[54:55] op_sel_hi:[1,0,0]\n"
            "v_cvt_pk_bf16_f32 v145, v130, v131\n"
            "v_pk_fma_f32 v[132:133], v[132:133], s[82:83], v[54:55] op_sel_hi:[1,0,0]\n"
            "v_dot2_f32_bf16 v53, v144, v144, 0\n"
            "v_cvt_pk_bf16_f32 v146, v132, v133\n"
            "v_pk_fma_f32 v[134:135], v[134:135], s[82:83], v[54:55] op_sel_hi:[1,0,0]\n"
            "v_dot2_f32_bf16 v53, v145, v145, v53\n"
            "v_cvt_pk_bf16_f32 v147, v134, v135\n"
            "v_pk_fma_f32 v[136:137], v[136:137], s[82:83], v[54:55] op_sel_hi:[1,0,0]\n"
            "v_dot2_f32_bf16 v53, v146, v146, v53\n"
            "v_cvt_pk_bf16_f32 v148, v136, v137\n"
            "v_pk_fma_f32 v[138:139], v[138:139], s[82:83], v[54:55] op_sel_hi:[1,0,0]\n"
            "v_dot2_f32_bf16 v53, v147, v147, v53\n"
            "v_cvt_pk_bf16_f32 v149, v138, v139\n"
            "v_pk_fma_f32 v[140:141], v[140:141], s[82:83], v[54:55] op_sel_hi:[1,0,0]\n"
            "v_pk_fma_f32 v[142:143], v[142:143], s[82:83], v[54:55] op_sel_hi:[1,0,0]\n"
            "v_dot2_f32_bf16 v53, v148, v148, v53\n"
            "v_cvt_pk_bf16_f32 v150, v140, v141\n"
            "v_cvt_pk_bf16_f32 v151, v142, v143\n"
            "v_dot2_f32_bf16 v53, v149, v149, v53\n"
            "v_and_b32 v151, v8, v151\n"
            "v_dot2_f32_bf16 v53, v150, v150, v53\n"
            "v_dot2_f32_bf16 v53, v151, v151, v53\n"
            "v_fmac_f32 v241, 0xbc924925, v240\n"
            "v_mul_f32 v251, v241, v250\n"
            "v_pk_fma_f32 v[238:239], v[238:239], v[250:251], v[250:251] op_sel:[0,0,1] op_sel_hi:[1,0,1]\n"
            "v_mfma_f32_16x16x4_f32 v[60:63], v182, v53, 0\n"
            "ds_write_b128 v188, v[144:147] offset:43328\n"
            "v_pk_fma_f32 v[224:225], v[224:225], v[250:251], v[250:251] op_sel:[0,0,1] op_sel_hi:[1,0,1]\n"
            "v_pk_fma_f32 v[226:227], v[226:227], v[250:251], v[250:251] op_sel:[0,0,1] op_sel_hi:[1,0,1]\n"
            "v_pk_fma_f32 v[228:229], v[228:229], v[250:251], v[250:251] op_sel:[0,0,1] op_sel_hi:[1,0,1]\n"
            "v_pk_fma_f32 v[230:231], v[230:231], v[250:251], v[250:251] op_sel:[0,0,1] op_sel_hi:[1,0,1]\n"
            "v_pk_fma_f32 v[232:233], v[232:233], v[250:251], v[250:251] op_sel:[0,0,1] op_sel_hi:[1,0,1]\n"
            "v_pk_fma_f32 v[234:235], v[234:235], v[250:251], v[250:251] op_sel:[0,0,1] op_sel_hi:[1,0,1]\n"
            "v_pk_fma_f32 v[236:237], v[236:237], v[250:251], v[250:251] op_sel:[0,0,1] op_sel_hi:[1,0,1]\n"
            "v_cvt_pk_bf16_f32 v207, v238, v239\n"
            "v_cvt_pk_bf16_f32 v200, v224, v225\n"
            "v_cmp_nge_f32 vcc, 0x43160000, v60\n"
            "v_cvt_pk_bf16_f32 v201, v226, v227\n"
            "v_cvt_pk_bf16_f32 v202, v228, v229\n"
            "v_cndmask_b32 v55, 0, v183, vcc\n"
            "v_or_b32 v151, v151, v55\n"
            "ds_write_b128 v188, v[148:151] offset:44352\n"
            "v_cvt_pk_bf16_f32 v203, v230, v231\n"
            "v_cvt_pk_bf16_f32 v204, v232, v233\n"
            "v_cvt_pk_bf16_f32 v205, v234, v235\n"
            "v_cvt_pk_bf16_f32 v206, v236, v237\n"
            "v_and_or_b32 v207, v207, v8, v183\n"
            "s_nop 1\n"
            "s_sub_u32 s60, s60, 1\n"
            "s_cbranch_scc1 22f\n"
            "v_mov_b64 v[144:145], v[152:153]\n"
            "v_mov_b64 v[146:147], v[154:155]\n"
            "v_mov_b64 v[148:149], v[156:157]\n"
            "v_mov_b64 v[150:151], v[158:159]\n"
            "v_swap_b32 v192, v200\n"
            "v_swap_b32 v193, v201\n"
            "v_swap_b32 v194, v202\n"
            "v_swap_b32 v195, v203\n"
            "v_swap_b32 v196, v204\n"
            "v_swap_b32 v197, v205\n"
            "v_swap_b32 v198, v206\n"
            "v_swap_b32 v199, v207\n"
            "v_mov_b32 v188, v5\n"
            "s_branch 23b\n"
            "22:\n"
            "s_mov_b64 exec, 1\n"
            "ds_add_u32 v160, v161 offset:244\n"
            "s_mov_b64 exec, -1\n"
            "s_mov_b32 s61, 1048576\n"
            "s_setprio 2\n"
            "ds_write_b128 v16, v[192:195] offset:20480\n"
            "ds_write_b128 v17, v[196:199] offset:20480\n"
            "ds_write_b128 v16, v[200:203] offset:22528\n"
            "ds_write_b128 v17, v[204:207] offset:22528\n"
            "ds_read_b64_tr_b16 v[224:225], v20 offset:20480\n"
            "ds_read_b64_tr_b16 v[226:227], v20 offset:22528\n"
            "ds_read_b64_tr_b16 v[232:233], v21 offset:20480\n"
            "ds_read_b64_tr_b16 v[234:235], v21 offset:22528\n"
            "ds_read_b64_tr_b16 v[240:241], v22 offset:20480\n"
            "ds_read_b64_tr_b16 v[242:243], v22 offset:22528\n"
            "ds_read_b64_tr_b16 v[248:249], v23 offset:20480\n"
            "ds_read_b64_tr_b16 v[250:251], v23 offset:22528\n"
            "v_mov_b64 v[184:185], 0\n"
            "v_mov_b64 v[186:187], 0\n"
            "v_mov_b64 v[188:189], 0\n"
            "v_mov_b64 v[190:191], 0\n"
            "v_mov_b64 v[174:175], 0\n"
            "v_mov_b64 v[228:229], 0\n"
            "v_mov_b64 v[230:231], 0\n"
            "v_mov_b64 v[236:237], 0\n"
            "v_mov_b64 v[238:239], 0\n"
            "v_mov_b64 v[244:245], 0\n"
            "v_mov_b64 v[246:247], 0\n"
            "v_mov_b64 v[252:253], 0\n"
            "v_mov_b64 v[254:255], 0\n"
            "s_cmp_eq_u32 %[wave], 3\n"
            "s_cbranch_scc1 2f\n"
            "s_sleep 4\n"
            "s_mov_b64 exec, 0xfff\n"
            "buffer_load_dwordx2 v[6:7], v4, s[40:43], 0 offen sc1\n"
            "s_mov_b64 exec, -1\n"
            "2:\n"
            "ds_read_b32 v162, v160 offset:244\n"
            "s_waitcnt lgkmcnt(0)\n"
            "v_cmp_gt_u32 vcc, 4, v162\n"
            "s_nop 4\n"
            "s_cbranch_vccz 17f\n"
            "s_sleep 1\n"
            "s_branch 2b\n"
            "17:\n"
            "ds_read_b128 v[24:27], v5 offset:43328\n"
            "ds_read_b128 v[28:31], v5 offset:44352\n"
            "ds_read_b128 v[32:35], v5 offset:45376\n"
            "ds_read_b128 v[36:39], v5 offset:46400\n"
            "ds_read_b128 v[40:43], v5 offset:47424\n"
            "ds_read_b128 v[44:47], v5 offset:48448\n"
            "ds_read_b128 v[48:51], v5 offset:49472\n"
            "ds_read_b128 v[52:55], v5 offset:50496\n"
            "ds_read_b128 v[56:59], v5 offset:51520\n"
            "ds_read_b128 v[60:63], v5 offset:52544\n"
            "s_mov_b64 exec, 0xfff\n"
            "s_waitcnt vmcnt(0)\n"
            "v_cmp_ne_u64 vcc, s[80:81], v[6:7]\n"
            "s_mov_b64 exec, -1\n"
            "s_nop 3\n"
            "s_cbranch_vccnz 28f\n"
            "s_add_u32 m0, s57, 0\n"
            "s_nop 0\n"
            "buffer_load_dwordx4 v18, s[44:47], m0 offen offset:0 sc1 lds\n"
            "buffer_load_dwordx4 v18, s[44:47], m0 offen offset:1024 sc1 lds\n"
            "buffer_load_dwordx4 v18, s[44:47], m0 offen offset:2048 sc1 lds\n"
            "buffer_load_dwordx4 v18, s[44:47], m0 offen offset:3072 sc1 lds\n"
            "s_add_u32 m0, s57, 4096\n"
            "s_nop 0\n"
            "buffer_load_dwordx4 v18, s[44:47], m0 offen offset:0 sc1 lds\n"
            "buffer_load_dwordx4 v18, s[44:47], m0 offen offset:1024 sc1 lds\n"
            "buffer_load_dwordx4 v18, s[44:47], m0 offen offset:2048 sc1 lds\n"
            "buffer_load_dwordx4 v18, s[44:47], m0 offen offset:3072 sc1 lds\n"
            "s_mov_b64 exec, 1\n"
            "ds_add_u32 v160, v161 offset:236\n"
            "s_mov_b64 exec, -1\n"
            "28:\n"
            "12:\n"
            "s_waitcnt lgkmcnt(0)\n"
            "v_mfma_f32_16x16x32_bf16 v[144:147], v[192:195], v[24:27], 0\n"
            "v_mfma_f32_16x16x32_bf16 v[144:147], v[196:199], v[28:31], v[144:147]\n"
            "v_mfma_f32_16x16x32_bf16 v[148:151], v[200:203], v[24:27], 0\n"
            "v_mfma_f32_16x16x32_bf16 v[148:151], v[204:207], v[28:31], v[148:151]\n"
            "s_nop 4\n"
            "v_mfma_f32_16x16x32_bf16 v[152:155], v[192:195], v[32:35], 0\n"
            "v_exp_f32 v144, v144\n"
            "v_mfma_f32_16x16x32_bf16 v[152:155], v[196:199], v[36:39], v[152:155]\n"
            "v_exp_f32 v145, v145\n"
            "v_mfma_f32_16x16x32_bf16 v[156:159], v[200:203], v[32:35], 0\n"
            "v_exp_f32 v146, v146\n"
            "v_cvt_pk_bf16_f32 v176, v144, v145\n"
            "v_mfma_f32_16x16x32_bf16 v[156:159], v[204:207], v[36:39], v[156:159]\n"
            "v_exp_f32 v147, v147\n"
            "v_exp_f32 v148, v148\n"
            "v_cvt_pk_bf16_f32 v177, v146, v147\n"
            "v_exp_f32 v149, v149\n"
            "v_exp_f32 v150, v150\n"
            "v_cvt_pk_bf16_f32 v178, v148, v149\n"
            "v_exp_f32 v151, v151\n"
            "s_nop 0\n"
            "v_cvt_pk_bf16_f32 v179, v150, v151\n"
            "v_mfma_f32_16x16x32_bf16 v[144:147], v[192:195], v[40:43], 0\n"
            "v_exp_f32 v152, v152\n"
            "v_mfma_f32_16x16x32_bf16 v[144:147], v[196:199], v[44:47], v[144:147]\n"
            "v_exp_f32 v153, v153\n"
            "v_mfma_f32_16x16x32_bf16 v[148:151], v[200:203], v[40:43], 0\n"
            "v_exp_f32 v154, v154\n"
            "v_cvt_pk_bf16_f32 v180, v152, v153\n"
            "v_mfma_f32_16x16x32_bf16 v[148:151], v[204:207], v[44:47], v[148:151]\n"
            "v_exp_f32 v155, v155\n"
            "v_mfma_f32_16x16x32_bf16 v[64:67], v[224:227], v[176:179], 0\n"
            "v_exp_f32 v156, v156\n"
            "v_cvt_pk_bf16_f32 v181, v154, v155\n"
            "v_mfma_f32_16x16x32_bf16 v[68:71], v[232:235], v[176:179], 0\n"
            "v_exp_f32 v157, v157\n"
            "v_mfma_f32_16x16x32_bf16 v[72:75], v[240:243], v[176:179], 0\n"
            "v_exp_f32 v158, v158\n"
            "v_cvt_pk_bf16_f32 v182, v156, v157\n"
            "v_mfma_f32_16x16x32_bf16 v[76:79], v[248:251], v[176:179], 0\n"
            "v_exp_f32 v159, v159\n"
            "s_nop 0\n"
            "v_cvt_pk_bf16_f32 v183, v158, v159\n"
            "s_cbranch_vccz 29f\n"
            "s_mov_b64 exec, 0xfff\n"
            "buffer_load_dwordx2 v[6:7], v4, s[40:43], 0 offen sc1\n"
            "s_mov_b64 exec, -1\n"
            "29:\n"
            "v_mfma_f32_16x16x32_bf16 v[152:155], v[192:195], v[48:51], 0\n"
            "v_exp_f32 v144, v144\n"
            "v_mfma_f32_16x16x32_bf16 v[152:155], v[196:199], v[52:55], v[152:155]\n"
            "v_exp_f32 v145, v145\n"
            "v_mfma_f32_16x16x32_bf16 v[156:159], v[200:203], v[48:51], 0\n"
            "v_exp_f32 v146, v146\n"
            "v_cvt_pk_bf16_f32 v176, v144, v145\n"
            "v_mfma_f32_16x16x32_bf16 v[156:159], v[204:207], v[52:55], v[156:159]\n"
            "v_exp_f32 v147, v147\n"
            "v_mfma_f32_16x16x32_bf16 v[80:83], v[224:227], v[180:183], 0\n"
            "v_exp_f32 v148, v148\n"
            "v_cvt_pk_bf16_f32 v177, v146, v147\n"
            "v_mfma_f32_16x16x32_bf16 v[84:87], v[232:235], v[180:183], 0\n"
            "v_exp_f32 v149, v149\n"
            "v_mfma_f32_16x16x32_bf16 v[88:91], v[240:243], v[180:183], 0\n"
            "v_exp_f32 v150, v150\n"
            "v_cvt_pk_bf16_f32 v178, v148, v149\n"
            "v_mfma_f32_16x16x32_bf16 v[92:95], v[248:251], v[180:183], 0\n"
            "v_exp_f32 v151, v151\n"
            "s_nop 0\n"
            "v_cvt_pk_bf16_f32 v179, v150, v151\n"
            "v_mfma_f32_16x16x32_bf16 v[144:147], v[192:195], v[56:59], 0\n"
            "v_exp_f32 v152, v152\n"
            "v_mfma_f32_16x16x32_bf16 v[144:147], v[196:199], v[60:63], v[144:147]\n"
            "v_exp_f32 v153, v153\n"
            "v_mfma_f32_16x16x32_bf16 v[148:151], v[200:203], v[56:59], 0\n"
            "v_exp_f32 v154, v154\n"
            "v_cvt_pk_bf16_f32 v180, v152, v153\n"
            "v_mfma_f32_16x16x32_bf16 v[148:151], v[204:207], v[60:63], v[148:151]\n"
            "v_exp_f32 v155, v155\n"
            "v_mfma_f32_16x16x32_bf16 v[96:99], v[224:227], v[176:179], 0\n"
            "v_exp_f32 v156, v156\n"
            "v_cvt_pk_bf16_f32 v181, v154, v155\n"
            "v_mfma_f32_16x16x32_bf16 v[100:103], v[232:235], v[176:179], 0\n"
            "v_exp_f32 v157, v157\n"
            "v_mfma_f32_16x16x32_bf16 v[104:107], v[240:243], v[176:179], 0\n"
            "v_exp_f32 v158, v158\n"
            "v_cvt_pk_bf16_f32 v182, v156, v157\n"
            "v_mfma_f32_16x16x32_bf16 v[108:111], v[248:251], v[176:179], 0\n"
            "v_exp_f32 v159, v159\n"
            "s_nop 0\n"
            "v_cvt_pk_bf16_f32 v183, v158, v159\n"
            "v_exp_f32 v144, v144\n"
            "v_exp_f32 v145, v145\n"
            "v_exp_f32 v146, v146\n"
            "v_cvt_pk_bf16_f32 v176, v144, v145\n"
            "v_exp_f32 v147, v147\n"
            "v_mfma_f32_16x16x32_bf16 v[112:115], v[224:227], v[180:183], 0\n"
            "v_exp_f32 v148, v148\n"
            "v_cvt_pk_bf16_f32 v177, v146, v147\n"
            "v_mfma_f32_16x16x32_bf16 v[116:119], v[232:235], v[180:183], 0\n"
            "v_exp_f32 v149, v149\n"
            "v_mfma_f32_16x16x32_bf16 v[120:123], v[240:243], v[180:183], 0\n"
            "v_exp_f32 v150, v150\n"
            "v_cvt_pk_bf16_f32 v178, v148, v149\n"
            "v_mfma_f32_16x16x32_bf16 v[124:127], v[248:251], v[180:183], 0\n"
            "v_exp_f32 v151, v151\n"
            "s_nop 0\n"
            "v_cvt_pk_bf16_f32 v179, v150, v151\n"
            "s_mov_b32 s60, 8\n"
            "s_nop 0\n"
            "v_mfma_f32_16x16x32_bf16 v[128:131], v[224:227], v[176:179], 0\n"
            "v_mfma_f32_16x16x32_bf16 v[132:135], v[232:235], v[176:179], 0\n"
            "v_mfma_f32_16x16x32_bf16 v[136:139], v[240:243], v[176:179], 0\n"
            "v_mfma_f32_16x16x32_bf16 v[140:143], v[248:251], v[176:179], 0\n"
            "s_cbranch_vccz 35f\n"
            "s_mov_b64 exec, 4095\n"
            "s_branch 5f\n"
            "4:\n"
            "buffer_load_dwordx2 v[6:7], v4, s[40:43], 0 offen sc1\n"
            "5:\n"
            "s_waitcnt vmcnt(0)\n"
            "v_cmp_ne_u64 vcc, s[80:81], v[6:7]\n"
            "s_sub_u32 s61, s61, 1\n"
            "s_cbranch_scc1 6f\n"
            "s_nop 2\n"
            "s_cbranch_vccnz 4b\n"
            "6:\n"
            "s_mov_b64 exec, -1\n"
            "s_add_u32 m0, s57, 0\n"
            "s_nop 0\n"
            "buffer_load_dwordx4 v18, s[44:47], m0 offen offset:0 sc1 lds\n"
            "buffer_load_dwordx4 v18, s[44:47], m0 offen offset:1024 sc1 lds\n"
            "buffer_load_dwordx4 v18, s[44:47], m0 offen offset:2048 sc1 lds\n"
            "buffer_load_dwordx4 v18, s[44:47], m0 offen offset:3072 sc1 lds\n"
            "s_add_u32 m0, s57, 4096\n"
            "s_nop 0\n"
            "buffer_load_dwordx4 v18, s[44:47], m0 offen offset:0 sc1 lds\n"
            "buffer_load_dwordx4 v18, s[44:47], m0 offen offset:1024 sc1 lds\n"
            "buffer_load_dwordx4 v18, s[44:47], m0 offen offset:2048 sc1 lds\n"
            "buffer_load_dwordx4 v18, s[44:47], m0 offen offset:3072 sc1 lds\n"
            "s_mov_b64 exec, 1\n"
            "ds_add_u32 v160, v161 offset:236\n"
            "s_mov_b64 exec, -1\n"
            "35:\n"
            "s_add_u32 m0, s57, 8192\n"
            "s_nop 0\n"
            "buffer_load_dwordx4 v18, s[44:47], m0 offen offset:0 sc1 lds\n"
            "buffer_load_dwordx4 v18, s[44:47], m0 offen offset:1024 sc1 lds\n"
            "buffer_load_dwordx4 v18, s[44:47], m0 offen offset:2048 sc1 lds\n"
            "buffer_load_dwordx4 v18, s[44:47], m0 offen offset:3072 sc1 lds\n"
            "s_add_u32 m0, s57, 12288\n"
            "s_nop 0\n"
            "buffer_load_dwordx4 v18, s[44:47], m0 offen offset:0 sc1 lds\n"
            "buffer_load_dwordx4 v18, s[44:47], m0 offen offset:1024 sc1 lds\n"
            "buffer_load_dwordx4 v18, s[44:47], m0 offen offset:2048 sc1 lds\n"
            "buffer_load_dwordx4 v18, s[44:47], m0 offen offset:3072 sc1 lds\n"
            "v_add_u32 v18, 16384, v18\n"
            "v_or3_b32 v162, v31, v39, v47\n"
            "v_or3_b32 v162, v162, v55, v63\n"
            "v_cmp_lt_u32 vcc, 0xffff, v162\n"
            "s_waitcnt vmcnt(8)\n"
            "s_nop 1\n"
            "s_setprio 0\n"
            "s_lshr_b32 s61, vcc_hi, 16\n"
            "s_cbranch_scc1 8f\n"
            "s_mov_b64 vcc, -1\n"
            "buffer_load_dwordx2 v[6:7], v4, s[40:43], 0 offen offset:768 sc1\n"
            "ds_read_b128 v[192:195], v16 offset:0\n"
            "ds_read_b128 v[196:199], v17 offset:0\n"
            "ds_read_b128 v[200:203], v16 offset:2048\n"
            "ds_read_b128 v[204:207], v17 offset:2048\n"
            "ds_read_b128 v[208:211], v16 offset:4096\n"
            "ds_read_b128 v[212:215], v17 offset:4096\n"
            "ds_read_b128 v[216:219], v16 offset:6144\n"
            "ds_read_b128 v[220:223], v17 offset:6144\n"
            "s_waitcnt lgkmcnt(6)\n"
            "v_mfma_f32_16x16x32_bf16 v[144:147], v[192:195], v[24:27], 0\n"
            "v_mfma_f32_16x16x32_bf16 v[144:147], v[196:199], v[28:31], v[144:147]\n"
            "s_waitcnt lgkmcnt(4)\n"
            "v_mfma_f32_16x16x32_bf16 v[148:151], v[200:203], v[24:27], 0\n"
            "v_mfma_f32_16x16x32_bf16 v[148:151], v[204:207], v[28:31], v[148:151]\n"
            "s_waitcnt lgkmcnt(2)\n"
            "v_mfma_f32_16x16x32_bf16 v[152:155], v[208:211], v[24:27], 0\n"
            "v_mfma_f32_16x16x32_bf16 v[152:155], v[212:215], v[28:31], v[152:155]\n"
            "s_waitcnt lgkmcnt(0)\n"
            "v_mfma_f32_16x16x32_bf16 v[156:159], v[216:219], v[24:27], 0\n"
            "v_mfma_f32_16x16x32_bf16 v[156:159], v[220:223], v[28:31], v[156:159]\n"
            "s_nop 1\n"
            "s_branch 11f\n"
            ".p2align 3\n"
            "1:\n"
            "v_exp_f32 v159, v159\n"
            "v_cvt_pk_bf16_f32 v182, v156, v157\n"
            "v_mfma_f32_16x16x32_bf16 v[144:147], v[192:195], v[32:35], 0\n"
            "v_exp_f32 v160, v160\n"
            "v_mfma_f32_16x16x32_bf16 v[144:147], v[196:199], v[36:39], v[144:147]\n"
            "v_exp_f32 v161, v161\n"
            "v_cvt_pk_bf16_f32 v183, v158, v159\n"
            "v_mfma_f32_16x16x32_bf16 v[148:151], v[200:203], v[32:35], 0\n"
            "v_exp_f32 v162, v162\n"
            "v_mfma_f32_16x16x32_bf16 v[148:151], v[204:207], v[36:39], v[148:151]\n"
            "v_exp_f32 v163, v163\n"
            "v_cvt_pk_bf16_f32 v184, v160, v161\n"
            "v_mfma_f32_16x16x32_bf16 v[152:155], v[208:211], v[32:35], 0\n"
            "v_exp_f32 v164, v164\n"
            "v_mfma_f32_16x16x32_bf16 v[152:155], v[212:215], v[36:39], v[152:155]\n"
            "v_exp_f32 v165, v165\n"
            "v_cvt_pk_bf16_f32 v185, v162, v163\n"
            "v_mfma_f32_16x16x32_bf16 v[156:159], v[216:219], v[32:35], 0\n"
            "v_exp_f32 v166, v166\n"
            "v_mfma_f32_16x16x32_bf16 v[156:159], v[220:223], v[36:39], v[156:159]\n"
            "v_exp_f32 v167, v167\n"
            "v_cvt_pk_bf16_f32 v186, v164, v165\n"
            "v_mfma_f32_16x16x32_bf16 v[128:131], v[224:227], v[176:179], v[128:131]\n"
            "v_exp_f32 v168, v168\n"
            "v_mfma_f32_16x16x32_bf16 v[128:131], v[228:231], v[180:183], v[128:131]\n"
            "v_exp_f32 v169, v169\n"
            "v_cvt_pk_bf16_f32 v187, v166, v167\n"
            "v_mfma_f32_16x16x32_bf16 v[132:135], v[236:239], v[180:183], v[132:135]\n"
            "v_exp_f32 v170, v170\n"
            "v_mfma_f32_16x16x32_bf16 v[132:135], v[232:235], v[176:179], v[132:135]\n"
            "ds_read_b64_tr_b16 v[224:225], v20 offset:8192\n"
            "ds_read_b64_tr_b16 v[226:227], v20 offset:10240\n"
            "ds_read_b64_tr_b16 v[228:229], v20 offset:12288\n"
            "ds_read_b64_tr_b16 v[230:231], v20 offset:14336\n"
            "v_exp_f32 v171, v171\n"
            "v_cvt_pk_bf16_f32 v188, v168, v169\n"
            "v_mfma_f32_16x16x32_bf16 v[136:139], v[240:243], v[176:179], v[136:139]\n"
            "v_exp_f32 v172, v172\n"
            "v_mfma_f32_16x16x32_bf16 v[136:139], v[244:247], v[180:183], v[136:139]\n"
            "ds_read_b64_tr_b16 v[232:233], v21 offset:8192\n"
            "ds_read_b64_tr_b16 v[234:235], v21 offset:10240\n"
            "ds_read_b64_tr_b16 v[236:237], v21 offset:12288\n"
            "ds_read_b64_tr_b16 v[238:239], v21 offset:14336\n"
            "v_exp_f32 v173, v173\n"
            "v_cvt_pk_bf16_f32 v189, v170, v171\n"
            "v_mfma_f32_16x16x32_bf16 v[140:143], v[252:255], v[180:183], v[140:143]\n"
            "v_exp_f32 v174, v174\n"
            "v_mfma_f32_16x16x32_bf16 v[140:143], v[248:251], v[176:179], v[140:143]\n"
            "ds_read_b64_tr_b16 v[240:241], v22 offset:8192\n"
            "ds_read_b64_tr_b16 v[242:243], v22 offset:10240\n"
            "ds_read_b64_tr_b16 v[244:245], v22 offset:12288\n"
            "ds_read_b64_tr_b16 v[246:247], v22 offset:14336\n"
            "v_exp_f32 v175, v175\n"
            "v_cvt_pk_bf16_f32 v190, v172, v173\n"
            "v_mfma_f32_16x16x32_bf16 v[160:163], v[192:195], v[40:43], 0\n"
            "v_exp_f32 v144, v144\n"
            "v_mfma_f32_16x16x32_bf16 v[160:163], v[196:199], v[44:47], v[160:163]\n"
            "ds_read_b64_tr_b16 v[248:249], v23 offset:8192\n"
            "ds_read_b64_tr_b16 v[250:251], v23 offset:10240\n"
            "ds_read_b64_tr_b16 v[252:253], v23 offset:12288\n"
            "ds_read_b64_tr_b16 v[254:255], v23 offset:14336\n"
            "v_exp_f32 v145, v145\n"
            "v_cvt_pk_bf16_f32 v191, v174, v175\n"
            "v_mfma_f32_16x16x32_bf16 v[164:167], v[200:203], v[40:43], 0\n"
            "v_exp_f32 v146, v146\n"
            "v_mfma_f32_16x16x32_bf16 v[164:167], v[204:207], v[44:47], v[164:167]\n"
            "v_exp_f32 v147, v147\n"
            "v_cvt_pk_bf16_f32 v176, v144, v145\n"
            "v_mfma_f32_16x16x32_bf16 v[168:171], v[208:211], v[40:43], 0\n"
            "v_exp_f32 v148, v148\n"
            "v_mfma_f32_16x16x32_bf16 v[168:171], v[212:215], v[44:47], v[168:171]\n"
            "v_exp_f32 v149, v149\n"
            "v_cvt_pk_bf16_f32 v177, v146, v147\n"
            "v_mfma_f32_16x16x32_bf16 v[172:175], v[216:219], v[40:43], 0\n"
            "v_exp_f32 v150, v150\n"
            "v_mfma_f32_16x16x32_bf16 v[172:175], v[220:223], v[44:47], v[172:175]\n"
            "s_waitcnt lgkmcnt(12)\n"
            "v_exp_f32 v151, v151\n"
            "v_cvt_pk_bf16_f32 v178, v148, v149\n"
            "v_mfma_f32_16x16x32_bf16 v[64:67], v[224:227], v[184:187], v[64:67]\n"
            "v_exp_f32 v152, v152\n"
            "v_mfma_f32_16x16x32_bf16 v[64:67], v[228:231], v[188:191], v[64:67]\n"
            "s_waitcnt lgkmcnt(8)\n"
            "v_exp_f32 v153, v153\n"
            "v_cvt_pk_bf16_f32 v179, v150, v151\n"
            "v_mfma_f32_16x16x32_bf16 v[68:71], v[236:239], v[188:191], v[68:71]\n"
            "v_exp_f32 v154, v154\n"
            "v_mfma_f32_16x16x32_bf16 v[68:71], v[232:235], v[184:187], v[68:71]\n"
            "s_waitcnt lgkmcnt(4)\n"
            "v_exp_f32 v155, v155\n"
            "v_cvt_pk_bf16_f32 v180, v152, v153\n"
            "v_mfma_f32_16x16x32_bf16 v[72:75], v[240:243], v[184:187], v[72:75]\n"
            "v_exp_f32 v156, v156\n"
            "v_mfma_f32_16x16x32_bf16 v[72:75], v[244:247], v[188:191], v[72:75]\n"
            "s_waitcnt lgkmcnt(0)\n"
            "v_exp_f32 v157, v157\n"
            "v_cvt_pk_bf16_f32 v181, v154, v155\n"
            "v_mfma_f32_16x16x32_bf16 v[76:79], v[252:255], v[188:191], v[76:79]\n"
            "v_exp_f32 v158, v158\n"
            "v_mfma_f32_16x16x32_bf16 v[76:79], v[248:251], v[184:187], v[76:79]\n"
            "v_exp_f32 v159, v159\n"
            "v_cvt_pk_bf16_f32 v182, v156, v157\n"
            "v_mfma_f32_16x16x32_bf16 v[144:147], v[192:195], v[48:51], 0\n"
            "v_exp_f32 v160, v160\n"
            "v_mfma_f32_16x16x32_bf16 v[144:147], v[196:199], v[52:55], v[144:147]\n"
            "v_exp_f32 v161, v161\n"
            "v_cvt_pk_bf16_f32 v183, v158, v159\n"
            "v_mfma_f32_16x16x32_bf16 v[148:151], v[200:203], v[48:51], 0\n"
            "v_exp_f32 v162, v162\n"
            "v_mfma_f32_16x16x32_bf16 v[148:151], v[204:207], v[52:55], v[148:151]\n"
            "v_exp_f32 v163, v163\n"
            "v_cvt_pk_bf16_f32 v184, v160, v161\n"
            "v_mfma_f32_16x16x32_bf16 v[152:155], v[208:211], v[48:51], 0\n"
            "v_exp_f32 v164, v164\n"
            "v_mfma_f32_16x16x32_bf16 v[152:155], v[212:215], v[52:55], v[152:155]\n"
            "v_exp_f32 v165, v165\n"
            "v_cvt_pk_bf16_f32 v185, v162, v163\n"
            "v_mfma_f32_16x16x32_bf16 v[156:159], v[216:219], v[48:51], 0\n"
            "v_exp_f32 v166, v166\n"
            "v_mfma_f32_16x16x32_bf16 v[156:159], v[220:223], v[52:55], v[156:159]\n"
            "v_exp_f32 v167, v167\n"
            "v_cvt_pk_bf16_f32 v186, v164, v165\n"
            "v_mfma_f32_16x16x32_bf16 v[80:83], v[224:227], v[176:179], v[80:83]\n"
            "v_exp_f32 v168, v168\n"
            "v_mfma_f32_16x16x32_bf16 v[80:83], v[228:231], v[180:183], v[80:83]\n"
            "v_exp_f32 v169, v169\n"
            "v_cvt_pk_bf16_f32 v187, v166, v167\n"
            "v_mfma_f32_16x16x32_bf16 v[84:87], v[236:239], v[180:183], v[84:87]\n"
            "v_exp_f32 v170, v170\n"
            "v_mfma_f32_16x16x32_bf16 v[84:87], v[232:235], v[176:179], v[84:87]\n"
            "v_exp_f32 v171, v171\n"
            "v_cvt_pk_bf16_f32 v188, v168, v169\n"
            "v_mfma_f32_16x16x32_bf16 v[88:91], v[240:243], v[176:179], v[88:91]\n"
            "v_exp_f32 v172, v172\n"
            "v_mfma_f32_16x16x32_bf16 v[88:91], v[244:247], v[180:183], v[88:91]\n"
            "v_exp_f32 v173, v173\n"
            "v_cvt_pk_bf16_f32 v189, v170, v171\n"
            "v_mfma_f32_16x16x32_bf16 v[92:95], v[252:255], v[180:183], v[92:95]\n"
            "v_exp_f32 v174, v174\n"
            "v_mfma_f32_16x16x32_bf16 v[92:95], v[248:251], v[176:179], v[92:95]\n"
            "v_exp_f32 v175, v175\n"
            "v_cvt_pk_bf16_f32 v190, v172, v173\n"
            "v_mfma_f32_16x16x32_bf16 v[160:163], v[192:195], v[56:59], 0\n"
            "v_exp_f32 v144, v144\n"
            "v_mfma_f32_16x16x32_bf16 v[160:163], v[196:199], v[60:63], v[160:163]\n"
            "s_waitcnt vmcnt(0)\n"
            "v_exp_f32 v145, v145\n"
            "v_cvt_pk_bf16_f32 v191, v174, v175\n"
            "v_mfma_f32_16x16x32_bf16 v[164:167], v[200:203], v[56:59], 0\n"
            "v_exp_f32 v146, v146\n"
            "v_mfma_f32_16x16x32_bf16 v[164:167], v[204:207], v[60:63], v[164:167]\n"
            "s_add_u32 m0, s57, 8192\n"
            "ds_read_b128 v[192:195], v16 offset:0\n"
            "buffer_load_dwordx4 v18, s[44:47], m0 offen offset:0 sc1 lds\n"
            "ds_read_b128 v[196:199], v17 offset:0\n"
            "buffer_load_dwordx4 v18, s[44:47], m0 offen offset:1024 sc1 lds\n"
            "v_exp_f32 v147, v147\n"
            "v_cvt_pk_bf16_f32 v176, v144, v145\n"
            "v_mfma_f32_16x16x32_bf16 v[168:171], v[208:211], v[56:59], 0\n"
            "v_exp_f32 v148, v148\n"
            "v_mfma_f32_16x16x32_bf16 v[168:171], v[212:215], v[60:63], v[168:171]\n"
            "ds_read_b128 v[200:203], v16 offset:2048\n"
            "buffer_load_dwordx4 v18, s[44:47], m0 offen offset:2048 sc1 lds\n"
            "ds_read_b128 v[204:207], v17 offset:2048\n"
            "buffer_load_dwordx4 v18, s[44:47], m0 offen offset:3072 sc1 lds\n"
            "v_exp_f32 v149, v149\n"
            "v_cvt_pk_bf16_f32 v177, v146, v147\n"
            "v_mfma_f32_16x16x32_bf16 v[172:175], v[216:219], v[56:59], 0\n"
            "v_exp_f32 v150, v150\n"
            "v_mfma_f32_16x16x32_bf16 v[172:175], v[220:223], v[60:63], v[172:175]\n"
            "s_add_u32 m0, s57, 12288\n"
            "ds_read_b128 v[208:211], v16 offset:4096\n"
            "buffer_load_dwordx4 v18, s[44:47], m0 offen offset:0 sc1 lds\n"
            "ds_read_b128 v[212:215], v17 offset:4096\n"
            "buffer_load_dwordx4 v18, s[44:47], m0 offen offset:1024 sc1 lds\n"
            "v_exp_f32 v151, v151\n"
            "v_cvt_pk_bf16_f32 v178, v148, v149\n"
            "v_mfma_f32_16x16x32_bf16 v[96:99], v[224:227], v[184:187], v[96:99]\n"
            "v_exp_f32 v152, v152\n"
            "v_mfma_f32_16x16x32_bf16 v[96:99], v[228:231], v[188:191], v[96:99]\n"
            "ds_read_b128 v[216:219], v16 offset:6144\n"
            "buffer_load_dwordx4 v18, s[44:47], m0 offen offset:2048 sc1 lds\n"
            "ds_read_b128 v[220:223], v17 offset:6144\n"
            "buffer_load_dwordx4 v18, s[44:47], m0 offen offset:3072 sc1 lds\n"
            "v_exp_f32 v153, v153\n"
            "v_cvt_pk_bf16_f32 v179, v150, v151\n"
            "v_mfma_f32_16x16x32_bf16 v[100:103], v[236:239], v[188:191], v[100:103]\n"
            "v_exp_f32 v154, v154\n"
            "v_mfma_f32_16x16x32_bf16 v[100:103], v[232:235], v[184:187], v[100:103]\n"
            "v_exp_f32 v155, v155\n"
            "v_cvt_pk_bf16_f32 v180, v152, v153\n"
            "v_mfma_f32_16x16x32_bf16 v[104:107], v[240:243], v[184:187], v[104:107]\n"
            "v_exp_f32 v156, v156\n"
            "v_mfma_f32_16x16x32_bf16 v[104:107], v[244:247], v[188:191], v[104:107]\n"
            "v_exp_f32 v157, v157\n"
            "v_cvt_pk_bf16_f32 v181, v154, v155\n"
            "v_mfma_f32_16x16x32_bf16 v[108:111], v[252:255], v[188:191], v[108:111]\n"
            "v_exp_f32 v158, v158\n"
            "v_mfma_f32_16x16x32_bf16 v[108:111], v[248:251], v[184:187], v[108:111]\n"
            "s_waitcnt lgkmcnt(6)\n"
            "v_exp_f32 v159, v159\n"
            "v_cvt_pk_bf16_f32 v182, v156, v157\n"
            "v_mfma_f32_16x16x32_bf16 v[144:147], v[192:195], v[24:27], 0\n"
            "v_exp_f32 v160, v160\n"
            "v_mfma_f32_16x16x32_bf16 v[144:147], v[196:199], v[28:31], v[144:147]\n"
            "s_waitcnt lgkmcnt(4)\n"
            "v_exp_f32 v161, v161\n"
            "v_cvt_pk_bf16_f32 v183, v158, v159\n"
            "v_mfma_f32_16x16x32_bf16 v[148:151], v[200:203], v[24:27], 0\n"
            "v_exp_f32 v162, v162\n"
            "v_mfma_f32_16x16x32_bf16 v[148:151], v[204:207], v[28:31], v[148:151]\n"
            "s_waitcnt lgkmcnt(2)\n"
            "v_exp_f32 v163, v163\n"
            "v_cvt_pk_bf16_f32 v184, v160, v161\n"
            "v_mfma_f32_16x16x32_bf16 v[152:155], v[208:211], v[24:27], 0\n"
            "v_exp_f32 v164, v164\n"
            "v_mfma_f32_16x16x32_bf16 v[152:155], v[212:215], v[28:31], v[152:155]\n"
            "s_waitcnt lgkmcnt(0)\n"
            "v_exp_f32 v165, v165\n"
            "v_cvt_pk_bf16_f32 v185, v162, v163\n"
            "v_mfma_f32_16x16x32_bf16 v[156:159], v[216:219], v[24:27], 0\n"
            "v_exp_f32 v166, v166\n"
            "v_mfma_f32_16x16x32_bf16 v[156:159], v[220:223], v[28:31], v[156:159]\n"
            "v_exp_f32 v167, v167\n"
            "v_cvt_pk_bf16_f32 v186, v164, v165\n"
            "v_mfma_f32_16x16x32_bf16 v[112:115], v[224:227], v[176:179], v[112:115]\n"
            "v_exp_f32 v168, v168\n"
            "v_mfma_f32_16x16x32_bf16 v[112:115], v[228:231], v[180:183], v[112:115]\n"
            "s_add_u32 s44, s44, 16384\n"
            "v_exp_f32 v169, v169\n"
            "v_cvt_pk_bf16_f32 v187, v166, v167\n"
            "v_mfma_f32_16x16x32_bf16 v[116:119], v[236:239], v[180:183], v[116:119]\n"
            "v_exp_f32 v170, v170\n"
            "v_mfma_f32_16x16x32_bf16 v[116:119], v[232:235], v[176:179], v[116:119]\n"
            "s_addc_u32 s45, s45, 0\n"
            "v_exp_f32 v171, v171\n"
            "v_cvt_pk_bf16_f32 v188, v168, v169\n"
            "v_mfma_f32_16x16x32_bf16 v[120:123], v[240:243], v[176:179], v[120:123]\n"
            "v_exp_f32 v172, v172\n"
            "v_mfma_f32_16x16x32_bf16 v[120:123], v[244:247], v[180:183], v[120:123]\n"
            "v_exp_f32 v173, v173\n"
            "v_cvt_pk_bf16_f32 v189, v170, v171\n"
            "v_mfma_f32_16x16x32_bf16 v[124:127], v[252:255], v[180:183], v[124:127]\n"
            "v_exp_f32 v174, v174\n"
            "v_mfma_f32_16x16x32_bf16 v[124:127], v[248:251], v[176:179], v[124:127]\n"
            "v_exp_f32 v175, v175\n"
            "v_cvt_pk_bf16_f32 v190, v172, v173\n"
            "11:\n"
            "v_mfma_f32_16x16x32_bf16 v[160:163], v[192:195], v[32:35], 0\n"
            "v_exp_f32 v144, v144\n"
            "v_mfma_f32_16x16x32_bf16 v[160:163], v[196:199], v[36:39], v[160:163]\n"
            "v_exp_f32 v145, v145\n"
            "v_cvt_pk_bf16_f32 v191, v174, v175\n"
            "v_mfma_f32_16x16x32_bf16 v[164:167], v[200:203], v[32:35], 0\n"
            "v_exp_f32 v146, v146\n"
            "v_mfma_f32_16x16x32_bf16 v[164:167], v[204:207], v[36:39], v[164:167]\n"
            "v_exp_f32 v147, v147\n"
            "v_cvt_pk_bf16_f32 v176, v144, v145\n"
            "v_mfma_f32_16x16x32_bf16 v[168:171], v[208:211], v[32:35], 0\n"
            "v_exp_f32 v148, v148\n"
            "v_mfma_f32_16x16x32_bf16 v[168:171], v[212:215], v[36:39], v[168:171]\n"
            "v_exp_f32 v149, v149\n"
            "v_cvt_pk_bf16_f32 v177, v146, v147\n"
            "v_mfma_f32_16x16x32_bf16 v[172:175], v[216:219], v[32:35], 0\n"
            "v_exp_f32 v150, v150\n"
            "v_mfma_f32_16x16x32_bf16 v[172:175], v[220:223], v[36:39], v[172:175]\n"
            "v_exp_f32 v151, v151\n"
            "v_cvt_pk_bf16_f32 v178, v148, v149\n"
            "v_mfma_f32_16x16x32_bf16 v[128:131], v[224:227], v[184:187], v[128:131]\n"
            "v_exp_f32 v152, v152\n"
            "v_mfma_f32_16x16x32_bf16 v[128:131], v[228:231], v[188:191], v[128:131]\n"
            "v_exp_f32 v153, v153\n"
            "v_cvt_pk_bf16_f32 v179, v150, v151\n"
            "v_mfma_f32_16x16x32_bf16 v[132:135], v[236:239], v[188:191], v[132:135]\n"
            "v_exp_f32 v154, v154\n"
            "v_mfma_f32_16x16x32_bf16 v[132:135], v[232:235], v[184:187], v[132:135]\n"
            "ds_read_b64_tr_b16 v[224:225], v20 offset:0\n"
            "ds_read_b64_tr_b16 v[226:227], v20 offset:2048\n"
            "ds_read_b64_tr_b16 v[228:229], v20 offset:4096\n"
            "ds_read_b64_tr_b16 v[230:231], v20 offset:6144\n"
            "v_exp_f32 v155, v155\n"
            "v_cvt_pk_bf16_f32 v180, v152, v153\n"
            "v_mfma_f32_16x16x32_bf16 v[136:139], v[240:243], v[184:187], v[136:139]\n"
            "v_exp_f32 v156, v156\n"
            "v_mfma_f32_16x16x32_bf16 v[136:139], v[244:247], v[188:191], v[136:139]\n"
            "ds_read_b64_tr_b16 v[232:233], v21 offset:0\n"
            "ds_read_b64_tr_b16 v[234:235], v21 offset:2048\n"
            "ds_read_b64_tr_b16 v[236:237], v21 offset:4096\n"
            "ds_read_b64_tr_b16 v[238:239], v21 offset:6144\n"
            "v_exp_f32 v157, v157\n"
            "v_cvt_pk_bf16_f32 v181, v154, v155\n"
            "v_mfma_f32_16x16x32_bf16 v[140:143], v[252:255], v[188:191], v[140:143]\n"
            "v_exp_f32 v158, v158\n"
            "v_mfma_f32_16x16x32_bf16 v[140:143], v[248:251], v[184:187], v[140:143]\n"
            "ds_read_b64_tr_b16 v[240:241], v22 offset:0\n"
            "ds_read_b64_tr_b16 v[242:243], v22 offset:2048\n"
            "ds_read_b64_tr_b16 v[244:245], v22 offset:4096\n"
            "ds_read_b64_tr_b16 v[246:247], v22 offset:6144\n"
            "v_exp_f32 v159, v159\n"
            "v_cvt_pk_bf16_f32 v182, v156, v157\n"
            "v_mfma_f32_16x16x32_bf16 v[144:147], v[192:195], v[40:43], 0\n"
            "v_exp_f32 v160, v160\n"
            "v_mfma_f32_16x16x32_bf16 v[144:147], v[196:199], v[44:47], v[144:147]\n"
            "ds_read_b64_tr_b16 v[248:249], v23 offset:0\n"
            "ds_read_b64_tr_b16 v[250:251], v23 offset:2048\n"
            "ds_read_b64_tr_b16 v[252:253], v23 offset:4096\n"
            "ds_read_b64_tr_b16 v[254:255], v23 offset:6144\n"
            "v_exp_f32 v161, v161\n"
            "v_cvt_pk_bf16_f32 v183, v158, v159\n"
            "v_mfma_f32_16x16x32_bf16 v[148:151], v[200:203], v[40:43], 0\n"
            "v_exp_f32 v162, v162\n"
            "v_mfma_f32_16x16x32_bf16 v[148:151], v[204:207], v[44:47], v[148:151]\n"
            "v_exp_f32 v163, v163\n"
            "v_cvt_pk_bf16_f32 v184, v160, v161\n"
            "v_mfma_f32_16x16x32_bf16 v[152:155], v[208:211], v[40:43], 0\n"
            "v_exp_f32 v164, v164\n"
            "v_mfma_f32_16x16x32_bf16 v[152:155], v[212:215], v[44:47], v[152:155]\n"
            "v_exp_f32 v165, v165\n"
            "v_cvt_pk_bf16_f32 v185, v162, v163\n"
            "v_mfma_f32_16x16x32_bf16 v[156:159], v[216:219], v[40:43], 0\n"
            "v_exp_f32 v166, v166\n"
            "v_mfma_f32_16x16x32_bf16 v[156:159], v[220:223], v[44:47], v[156:159]\n"
            "s_waitcnt lgkmcnt(12)\n"
            "v_exp_f32 v167, v167\n"
            "v_cvt_pk_bf16_f32 v186, v164, v165\n"
            "v_mfma_f32_16x16x32_bf16 v[64:67], v[224:227], v[176:179], v[64:67]\n"
            "v_exp_f32 v168, v168\n"
            "v_mfma_f32_16x16x32_bf16 v[64:67], v[228:231], v[180:183], v[64:67]\n"
            "s_waitcnt lgkmcnt(8)\n"
            "v_exp_f32 v169, v169\n"
            "v_cvt_pk_bf16_f32 v187, v166, v167\n"
            "v_mfma_f32_16x16x32_bf16 v[68:71], v[236:239], v[180:183], v[68:71]\n"
            "v_exp_f32 v170, v170\n"
            "v_mfma_f32_16x16x32_bf16 v[68:71], v[232:235], v[176:179], v[68:71]\n"
            "s_waitcnt lgkmcnt(4)\n"
            "v_exp_f32 v171, v171\n"
            "v_cvt_pk_bf16_f32 v188, v168, v169\n"
            "v_mfma_f32_16x16x32_bf16 v[72:75], v[240:243], v[176:179], v[72:75]\n"
            "v_exp_f32 v172, v172\n"
            "v_mfma_f32_16x16x32_bf16 v[72:75], v[244:247], v[180:183], v[72:75]\n"
            "s_waitcnt lgkmcnt(0)\n"
            "v_exp_f32 v173, v173\n"
            "v_cvt_pk_bf16_f32 v189, v170, v171\n"
            "v_mfma_f32_16x16x32_bf16 v[76:79], v[252:255], v[180:183], v[76:79]\n"
            "v_exp_f32 v174, v174\n"
            "v_mfma_f32_16x16x32_bf16 v[76:79], v[248:251], v[176:179], v[76:79]\n"
            "v_exp_f32 v175, v175\n"
            "v_cvt_pk_bf16_f32 v190, v172, v173\n"
            "v_mfma_f32_16x16x32_bf16 v[160:163], v[192:195], v[48:51], 0\n"
            "v_exp_f32 v144, v144\n"
            "v_mfma_f32_16x16x32_bf16 v[160:163], v[196:199], v[52:55], v[160:163]\n"
            "v_exp_f32 v145, v145\n"
            "v_cvt_pk_bf16_f32 v191, v174, v175\n"
            "v_mfma_f32_16x16x32_bf16 v[164:167], v[200:203], v[48:51], 0\n"
            "v_exp_f32 v146, v146\n"
            "v_mfma_f32_16x16x32_bf16 v[164:167], v[204:207], v[52:55], v[164:167]\n"
            "v_exp_f32 v147, v147\n"
            "v_cvt_pk_bf16_f32 v176, v144, v145\n"
            "v_mfma_f32_16x16x32_bf16 v[168:171], v[208:211], v[48:51], 0\n"
            "v_exp_f32 v148, v148\n"
            "v_mfma_f32_16x16x32_bf16 v[168:171], v[212:215], v[52:55], v[168:171]\n"
            "v_exp_f32 v149, v149\n"
            "v_cvt_pk_bf16_f32 v177, v146, v147\n"
            "v_mfma_f32_16x16x32_bf16 v[172:175], v[216:219], v[48:51], 0\n"
            "v_exp_f32 v150, v150\n"
            "v_mfma_f32_16x16x32_bf16 v[172:175], v[220:223], v[52:55], v[172:175]\n"
            "v_exp_f32 v151, v151\n"
            "v_cvt_pk_bf16_f32 v178, v148, v149\n"
            "v_mfma_f32_16x16x32_bf16 v[80:83], v[224:227], v[184:187], v[80:83]\n"
            "v_exp_f32 v152, v152\n"
            "v_mfma_f32_16x16x32_bf16 v[80:83], v[228:231], v[188:191], v[80:83]\n"
            "v_exp_f32 v153, v153\n"
            "v_cvt_pk_bf16_f32 v179, v150, v151\n"
            "v_mfma_f32_16x16x32_bf16 v[84:87], v[236:239], v[188:191], v[84:87]\n"
            "v_exp_f32 v154, v154\n"
            "v_mfma_f32_16x16x32_bf16 v[84:87], v[232:235], v[184:187], v[84:87]\n"
            "v_exp_f32 v155, v155\n"
            "v_cvt_pk_bf16_f32 v180, v152, v153\n"
            "v_mfma_f32_16x16x32_bf16 v[88:91], v[240:243], v[184:187], v[88:91]\n"
            "v_exp_f32 v156, v156\n"
            "v_mfma_f32_16x16x32_bf16 v[88:91], v[244:247], v[188:191], v[88:91]\n"
            "v_exp_f32 v157, v157\n"
            "v_cvt_pk_bf16_f32 v181, v154, v155\n"
            "v_mfma_f32_16x16x32_bf16 v[92:95], v[252:255], v[188:191], v[92:95]\n"
            "v_exp_f32 v158, v158\n"
            "v_mfma_f32_16x16x32_bf16 v[92:95], v[248:251], v[184:187], v[92:95]\n"
            "s_cbranch_vccnz 14f\n"
            "15:\n"
            "v_exp_f32 v159, v159\n"
            "v_cvt_pk_bf16_f32 v182, v156, v157\n"
            "v_mfma_f32_16x16x32_bf16 v[144:147], v[192:195], v[56:59], 0\n"
            "v_exp_f32 v160, v160\n"
            "v_mfma_f32_16x16x32_bf16 v[144:147], v[196:199], v[60:63], v[144:147]\n"
            "s_waitcnt vmcnt(0)\n"
            "v_exp_f32 v161, v161\n"
            "v_cvt_pk_bf16_f32 v183, v158, v159\n"
            "v_mfma_f32_16x16x32_bf16 v[148:151], v[200:203], v[56:59], 0\n"
            "v_exp_f32 v162, v162\n"
            "v_mfma_f32_16x16x32_bf16 v[148:151], v[204:207], v[60:63], v[148:151]\n"
            "s_add_u32 m0, s57, 0\n"
            "ds_read_b128 v[192:195], v16 offset:8192\n"
            "buffer_load_dwordx4 v18, s[44:47], m0 offen offset:0 sc1 lds\n"
            "ds_read_b128 v[196:199], v17 offset:8192\n"
            "buffer_load_dwordx4 v18, s[44:47], m0 offen offset:1024 sc1 lds\n"
            "v_exp_f32 v163, v163\n"
            "v_cvt_pk_bf16_f32 v184, v160, v161\n"
            "v_mfma_f32_16x16x32_bf16 v[152:155], v[208:211], v[56:59], 0\n"
            "v_exp_f32 v164, v164\n"
            "v_mfma_f32_16x16x32_bf16 v[152:155], v[212:215], v[60:63], v[152:155]\n"
            "ds_read_b128 v[200:203], v16 offset:10240\n"
            "buffer_load_dwordx4 v18, s[44:47], m0 offen offset:2048 sc1 lds\n"
            "ds_read_b128 v[204:207], v17 offset:10240\n"
            "buffer_load_dwordx4 v18, s[44:47], m0 offen offset:3072 sc1 lds\n"
            "v_exp_f32 v165, v165\n"
            "v_cvt_pk_bf16_f32 v185, v162, v163\n"
            "v_mfma_f32_16x16x32_bf16 v[156:159], v[216:219], v[56:59], 0\n"
            "v_exp_f32 v166, v166\n"
            "v_mfma_f32_16x16x32_bf16 v[156:159], v[220:223], v[60:63], v[156:159]\n"
            "s_add_u32 m0, s57, 4096\n"
            "ds_read_b128 v[208:211], v16 offset:12288\n"
            "buffer_load_dwordx4 v18, s[44:47], m0 offen offset:0 sc1 lds\n"
            "ds_read_b128 v[212:215], v17 offset:12288\n"
            "buffer_load_dwordx4 v18, s[44:47], m0 offen offset:1024 sc1 lds\n"
            "v_exp_f32 v167, v167\n"
            "v_cvt_pk_bf16_f32 v186, v164, v165\n"
            "v_mfma_f32_16x16x32_bf16 v[96:99], v[224:227], v[176:179], v[96:99]\n"
            "v_exp_f32 v168, v168\n"
            "v_mfma_f32_16x16x32_bf16 v[96:99], v[228:231], v[180:183], v[96:99]\n"
            "ds_read_b128 v[216:219], v16 offset:14336\n"
            "buffer_load_dwordx4 v18, s[44:47], m0 offen offset:2048 sc1 lds\n"
            "ds_read_b128 v[220:223], v17 offset:14336\n"
            "buffer_load_dwordx4 v18, s[44:47], m0 offen offset:3072 sc1 lds\n"
            "v_exp_f32 v169, v169\n"
            "v_cvt_pk_bf16_f32 v187, v166, v167\n"
            "v_mfma_f32_16x16x32_bf16 v[100:103], v[236:239], v[180:183], v[100:103]\n"
            "v_exp_f32 v170, v170\n"
            "v_mfma_f32_16x16x32_bf16 v[100:103], v[232:235], v[176:179], v[100:103]\n"
            "v_exp_f32 v171, v171\n"
            "v_cvt_pk_bf16_f32 v188, v168, v169\n"
            "v_mfma_f32_16x16x32_bf16 v[104:107], v[240:243], v[176:179], v[104:107]\n"
            "v_exp_f32 v172, v172\n"
            "v_mfma_f32_16x16x32_bf16 v[104:107], v[244:247], v[180:183], v[104:107]\n"
            "v_exp_f32 v173, v173\n"
            "v_cvt_pk_bf16_f32 v189, v170, v171\n"
            "v_mfma_f32_16x16x32_bf16 v[108:111], v[252:255], v[180:183], v[108:111]\n"
            "v_exp_f32 v174, v174\n"
            "v_mfma_f32_16x16x32_bf16 v[108:111], v[248:251], v[176:179], v[108:111]\n"
            "s_waitcnt lgkmcnt(6)\n"
            "v_exp_f32 v175, v175\n"
            "v_cvt_pk_bf16_f32 v190, v172, v173\n"
            "v_mfma_f32_16x16x32_bf16 v[160:163], v[192:195], v[24:27], 0\n"
            "v_exp_f32 v144, v144\n"
            "v_mfma_f32_16x16x32_bf16 v[160:163], v[196:199], v[28:31], v[160:163]\n"
            "s_waitcnt lgkmcnt(4)\n"
            "v_exp_f32 v145, v145\n"
            "v_cvt_pk_bf16_f32 v191, v174, v175\n"
            "v_mfma_f32_16x16x32_bf16 v[164:167], v[200:203], v[24:27], 0\n"
            "v_exp_f32 v146, v146\n"
            "v_mfma_f32_16x16x32_bf16 v[164:167], v[204:207], v[28:31], v[164:167]\n"
            "s_waitcnt lgkmcnt(2)\n"
            "v_exp_f32 v147, v147\n"
            "v_cvt_pk_bf16_f32 v176, v144, v145\n"
            "v_mfma_f32_16x16x32_bf16 v[168:171], v[208:211], v[24:27], 0\n"
            "v_exp_f32 v148, v148\n"
            "v_mfma_f32_16x16x32_bf16 v[168:171], v[212:215], v[28:31], v[168:171]\n"
            "s_waitcnt lgkmcnt(0)\n"
            "v_exp_f32 v149, v149\n"
            "v_cvt_pk_bf16_f32 v177, v146, v147\n"
            "v_mfma_f32_16x16x32_bf16 v[172:175], v[216:219], v[24:27], 0\n"
            "v_exp_f32 v150, v150\n"
            "v_mfma_f32_16x16x32_bf16 v[172:175], v[220:223], v[28:31], v[172:175]\n"
            "v_exp_f32 v151, v151\n"
            "v_cvt_pk_bf16_f32 v178, v148, v149\n"
            "v_mfma_f32_16x16x32_bf16 v[112:115], v[224:227], v[184:187], v[112:115]\n"
            "v_exp_f32 v152, v152\n"
            "v_mfma_f32_16x16x32_bf16 v[112:115], v[228:231], v[188:191], v[112:115]\n"
            "v_exp_f32 v153, v153\n"
            "v_cvt_pk_bf16_f32 v179, v150, v151\n"
            "v_mfma_f32_16x16x32_bf16 v[116:119], v[236:239], v[188:191], v[116:119]\n"
            "v_exp_f32 v154, v154\n"
            "v_mfma_f32_16x16x32_bf16 v[116:119], v[232:235], v[184:187], v[116:119]\n"
            "v_exp_f32 v155, v155\n"
            "v_cvt_pk_bf16_f32 v180, v152, v153\n"
            "v_mfma_f32_16x16x32_bf16 v[120:123], v[240:243], v[184:187], v[120:123]\n"
            "v_exp_f32 v156, v156\n"
            "v_mfma_f32_16x16x32_bf16 v[120:123], v[244:247], v[188:191], v[120:123]\n"
            "s_sub_u32 s60, s60, 1\n"
            "v_exp_f32 v157, v157\n"
            "v_cvt_pk_bf16_f32 v181, v154, v155\n"
            "v_mfma_f32_16x16x32_bf16 v[124:127], v[252:255], v[188:191], v[124:127]\n"
            "v_exp_f32 v158, v158\n"
            "v_mfma_f32_16x16x32_bf16 v[124:127], v[248:251], v[184:187], v[124:127]\n"
            "s_cbranch_scc0 1b\n"
            "v_exp_f32 v159, v159\n"
            "v_cvt_pk_bf16_f32 v182, v156, v157\n"
            "v_mfma_f32_16x16x32_bf16 v[144:147], v[192:195], v[32:35], 0\n"
            "v_exp_f32 v160, v160\n"
            "v_mfma_f32_16x16x32_bf16 v[144:147], v[196:199], v[36:39], v[144:147]\n"
            "v_exp_f32 v161, v161\n"
            "v_cvt_pk_bf16_f32 v183, v158, v159\n"
            "v_mfma_f32_16x16x32_bf16 v[148:151], v[200:203], v[32:35], 0\n"
            "v_exp_f32 v162, v162\n"
            "v_mfma_f32_16x16x32_bf16 v[148:151], v[204:207], v[36:39], v[148:151]\n"
            "v_exp_f32 v163, v163\n"
            "v_cvt_pk_bf16_f32 v184, v160, v161\n"
            "v_mfma_f32_16x16x32_bf16 v[152:155], v[208:211], v[32:35], 0\n"
            "v_exp_f32 v164, v164\n"
            "v_mfma_f32_16x16x32_bf16 v[152:155], v[212:215], v[36:39], v[152:155]\n"
            "v_exp_f32 v165, v165\n"
            "v_cvt_pk_bf16_f32 v185, v162, v163\n"
            "v_mfma_f32_16x16x32_bf16 v[156:159], v[216:219], v[32:35], 0\n"
            "v_exp_f32 v166, v166\n"
            "v_mfma_f32_16x16x32_bf16 v[156:159], v[220:223], v[36:39], v[156:159]\n"
            "v_exp_f32 v167, v167\n"
            "v_cvt_pk_bf16_f32 v186, v164, v165\n"
            "v_mfma_f32_16x16x32_bf16 v[128:131], v[224:227], v[176:179], v[128:131]\n"
            "v_exp_f32 v168, v168\n"
            "v_mfma_f32_16x16x32_bf16 v[128:131], v[228:231], v[180:183], v[128:131]\n"
            "v_exp_f32 v169, v169\n"
            "v_cvt_pk_bf16_f32 v187, v166, v167\n"
            "v_mfma_f32_16x16x32_bf16 v[132:135], v[236:239], v[180:183], v[132:135]\n"
            "v_exp_f32 v170, v170\n"
            "v_mfma_f32_16x16x32_bf16 v[132:135], v[232:235], v[176:179], v[132:135]\n"
            "ds_read_b64_tr_b16 v[224:225], v20 offset:8192\n"
            "ds_read_b64_tr_b16 v[226:227], v20 offset:10240\n"
            "ds_read_b64_tr_b16 v[228:229], v20 offset:12288\n"
            "ds_read_b64_tr_b16 v[230:231], v20 offset:14336\n"
            "v_exp_f32 v171, v171\n"
            "v_cvt_pk_bf16_f32 v188, v168, v169\n"
            "v_mfma_f32_16x16x32_bf16 v[136:139], v[240:243], v[176:179], v[136:139]\n"
            "v_exp_f32 v172, v172\n"
            "v_mfma_f32_16x16x32_bf16 v[136:139], v[244:247], v[180:183], v[136:139]\n"
            "ds_read_b64_tr_b16 v[232:233], v21 offset:8192\n"
            "ds_read_b64_tr_b16 v[234:235], v21 offset:10240\n"
            "ds_read_b64_tr_b16 v[236:237], v21 offset:12288\n"
            "ds_read_b64_tr_b16 v[238:239], v21 offset:14336\n"
            "v_exp_f32 v173, v173\n"
            "v_cvt_pk_bf16_f32 v189, v170, v171\n"
            "v_mfma_f32_16x16x32_bf16 v[140:143], v[252:255], v[180:183], v[140:143]\n"
            "v_exp_f32 v174, v174\n"
            "v_mfma_f32_16x16x32_bf16 v[140:143], v[248:251], v[176:179], v[140:143]\n"
            "ds_read_b64_tr_b16 v[240:241], v22 offset:8192\n"
            "ds_read_b64_tr_b16 v[242:243], v22 offset:10240\n"
            "ds_read_b64_tr_b16 v[244:245], v22 offset:12288\n"
            "ds_read_b64_tr_b16 v[246:247], v22 offset:14336\n"
            "v_exp_f32 v175, v175\n"
            "v_cvt_pk_bf16_f32 v190, v172, v173\n"
            "v_mfma_f32_16x16x32_bf16 v[160:163], v[192:195], v[40:43], 0\n"
            "v_exp_f32 v144, v144\n"
            "v_mfma_f32_16x16x32_bf16 v[160:163], v[196:199], v[44:47], v[160:163]\n"
            "s_waitcnt vmcnt(0)\n"
            "ds_read_b64_tr_b16 v[248:249], v23 offset:8192\n"
            "ds_read_b64_tr_b16 v[250:251], v23 offset:10240\n"
            "ds_read_b64_tr_b16 v[252:253], v23 offset:12288\n"
            "ds_read_b64_tr_b16 v[254:255], v23 offset:14336\n"
            "v_exp_f32 v145, v145\n"
            "v_cvt_pk_bf16_f32 v191, v174, v175\n"
            "v_mfma_f32_16x16x32_bf16 v[164:167], v[200:203], v[40:43], 0\n"
            "v_exp_f32 v146, v146\n"
            "v_mfma_f32_16x16x32_bf16 v[164:167], v[204:207], v[44:47], v[164:167]\n"
            "ds_read_b128 v[192:195], v16 offset:0\n"
            "ds_read_b128 v[196:199], v17 offset:0\n"
            "v_exp_f32 v147, v147\n"
            "v_cvt_pk_bf16_f32 v176, v144, v145\n"
            "v_mfma_f32_16x16x32_bf16 v[168:171], v[208:211], v[40:43], 0\n"
            "v_exp_f32 v148, v148\n"
            "v_mfma_f32_16x16x32_bf16 v[168:171], v[212:215], v[44:47], v[168:171]\n"
            "ds_read_b128 v[200:203], v16 offset:2048\n"
            "ds_read_b128 v[204:207], v17 offset:2048\n"
            "v_exp_f32 v149, v149\n"
            "v_cvt_pk_bf16_f32 v177, v146, v147\n"
            "v_mfma_f32_16x16x32_bf16 v[172:175], v[216:219], v[40:43], 0\n"
            "v_exp_f32 v150, v150\n"
            "v_mfma_f32_16x16x32_bf16 v[172:175], v[220:223], v[44:47], v[172:175]\n"
            "s_waitcnt lgkmcnt(15)\n"
            "ds_read_b128 v[208:211], v16 offset:4096\n"
            "ds_read_b128 v[212:215], v17 offset:4096\n"
            "v_exp_f32 v151, v151\n"
            "v_cvt_pk_bf16_f32 v178, v148, v149\n"
            "v_mfma_f32_16x16x32_bf16 v[64:67], v[224:227], v[184:187], v[64:67]\n"
            "v_exp_f32 v152, v152\n"
            "v_mfma_f32_16x16x32_bf16 v[64:67], v[228:231], v[188:191], v[64:67]\n"
            "s_waitcnt lgkmcnt(14)\n"
            "ds_read_b128 v[216:219], v16 offset:6144\n"
            "ds_read_b128 v[220:223], v17 offset:6144\n"
            "v_exp_f32 v153, v153\n"
            "v_cvt_pk_bf16_f32 v179, v150, v151\n"
            "v_mfma_f32_16x16x32_bf16 v[68:71], v[236:239], v[188:191], v[68:71]\n"
            "v_exp_f32 v154, v154\n"
            "v_mfma_f32_16x16x32_bf16 v[68:71], v[232:235], v[184:187], v[68:71]\n"
            "s_waitcnt lgkmcnt(12)\n"
            "v_exp_f32 v155, v155\n"
            "v_cvt_pk_bf16_f32 v180, v152, v153\n"
            "v_mfma_f32_16x16x32_bf16 v[72:75], v[240:243], v[184:187], v[72:75]\n"
            "v_exp_f32 v156, v156\n"
            "v_mfma_f32_16x16x32_bf16 v[72:75], v[244:247], v[188:191], v[72:75]\n"
            "s_waitcnt lgkmcnt(8)\n"
            "v_exp_f32 v157, v157\n"
            "v_cvt_pk_bf16_f32 v181, v154, v155\n"
            "v_mfma_f32_16x16x32_bf16 v[76:79], v[252:255], v[188:191], v[76:79]\n"
            "v_exp_f32 v158, v158\n"
            "v_mfma_f32_16x16x32_bf16 v[76:79], v[248:251], v[184:187], v[76:79]\n"
            "s_waitcnt lgkmcnt(6)\n"
            "v_exp_f32 v159, v159\n"
            "v_cvt_pk_bf16_f32 v182, v156, v157\n"
            "v_mfma_f32_16x16x32_bf16 v[144:147], v[192:195], v[24:27], 0\n"
            "v_exp_f32 v160, v160\n"
            "v_mfma_f32_16x16x32_bf16 v[144:147], v[196:199], v[28:31], v[144:147]\n"
            "s_waitcnt lgkmcnt(4)\n"
            "v_exp_f32 v161, v161\n"
            "v_cvt_pk_bf16_f32 v183, v158, v159\n"
            "v_mfma_f32_16x16x32_bf16 v[148:151], v[200:203], v[24:27], 0\n"
            "v_exp_f32 v162, v162\n"
            "v_mfma_f32_16x16x32_bf16 v[148:151], v[204:207], v[28:31], v[148:151]\n"
            "s_waitcnt lgkmcnt(2)\n"
            "v_exp_f32 v163, v163\n"
            "v_cvt_pk_bf16_f32 v184, v160, v161\n"
            "v_mfma_f32_16x16x32_bf16 v[152:155], v[208:211], v[24:27], 0\n"
            "v_exp_f32 v164, v164\n"
            "v_mfma_f32_16x16x32_bf16 v[152:155], v[212:215], v[28:31], v[152:155]\n"
            "s_waitcnt lgkmcnt(0)\n"
            "v_exp_f32 v165, v165\n"
            "v_cvt_pk_bf16_f32 v185, v162, v163\n"
            "v_mfma_f32_16x16x32_bf16 v[156:159], v[216:219], v[24:27], 0\n"
            "v_exp_f32 v166, v166\n"
            "v_mfma_f32_16x16x32_bf16 v[156:159], v[220:223], v[28:31], v[156:159]\n"
            "v_exp_f32 v167, v167\n"
            "v_cvt_pk_bf16_f32 v186, v164, v165\n"
            "v_mfma_f32_16x16x32_bf16 v[80:83], v[224:227], v[176:179], v[80:83]\n"
            "v_exp_f32 v168, v168\n"
            "v_mfma_f32_16x16x32_bf16 v[80:83], v[228:231], v[180:183], v[80:83]\n"
            "v_exp_f32 v169, v169\n"
            "v_cvt_pk_bf16_f32 v187, v166, v167\n"
            "v_mfma_f32_16x16x32_bf16 v[84:87], v[236:239], v[180:183], v[84:87]\n"
            "v_exp_f32 v170, v170\n"
            "v_mfma_f32_16x16x32_bf16 v[84:87], v[232:235], v[176:179], v[84:87]\n"
            "v_exp_f32 v171, v171\n"
            "v_cvt_pk_bf16_f32 v188, v168, v169\n"
            "v_mfma_f32_16x16x32_bf16 v[88:91], v[240:243], v[176:179], v[88:91]\n"
            "v_exp_f32 v172, v172\n"
            "v_mfma_f32_16x16x32_bf16 v[88:91], v[244:247], v[180:183], v[88:91]\n"
            "v_exp_f32 v173, v173\n"
            "v_cvt_pk_bf16_f32 v189, v170, v171\n"
            "v_mfma_f32_16x16x32_bf16 v[92:95], v[252:255], v[180:183], v[92:95]\n"
            "v_exp_f32 v174, v174\n"
            "v_mfma_f32_16x16x32_bf16 v[92:95], v[248:251], v[176:179], v[92:95]\n"
            "v_exp_f32 v175, v175\n"
            "v_cvt_pk_bf16_f32 v190, v172, v173\n"
            "v_mfma_f32_16x16x32_bf16 v[160:163], v[192:195], v[32:35], 0\n"
            "v_exp_f32 v144, v144\n"
            "v_mfma_f32_16x16x32_bf16 v[160:163], v[196:199], v[36:39], v[160:163]\n"
            "v_exp_f32 v145, v145\n"
            "v_cvt_pk_bf16_f32 v191, v174, v175\n"
            "v_mfma_f32_16x16x32_bf16 v[164:167], v[200:203], v[32:35], 0\n"
            "v_exp_f32 v146, v146\n"
            "v_mfma_f32_16x16x32_bf16 v[164:167], v[204:207], v[36:39], v[164:167]\n"
            "v_exp_f32 v147, v147\n"
            "v_cvt_pk_bf16_f32 v176, v144, v145\n"
            "v_mfma_f32_16x16x32_bf16 v[168:171], v[208:211], v[32:35], 0\n"
            "v_exp_f32 v148, v148\n"
            "v_mfma_f32_16x16x32_bf16 v[168:171], v[212:215], v[36:39], v[168:171]\n"
            "v_exp_f32 v149, v149\n"
            "v_cvt_pk_bf16_f32 v177, v146, v147\n"
            "v_mfma_f32_16x16x32_bf16 v[172:175], v[216:219], v[32:35], 0\n"
            "v_exp_f32 v150, v150\n"
            "v_mfma_f32_16x16x32_bf16 v[172:175], v[220:223], v[36:39], v[172:175]\n"
            "v_exp_f32 v151, v151\n"
            "v_cvt_pk_bf16_f32 v178, v148, v149\n"
            "v_mfma_f32_16x16x32_bf16 v[96:99], v[224:227], v[184:187], v[96:99]\n"
            "v_exp_f32 v152, v152\n"
            "v_mfma_f32_16x16x32_bf16 v[96:99], v[228:231], v[188:191], v[96:99]\n"
            "v_exp_f32 v153, v153\n"
            "v_cvt_pk_bf16_f32 v179, v150, v151\n"
            "v_mfma_f32_16x16x32_bf16 v[100:103], v[236:239], v[188:191], v[100:103]\n"
            "v_exp_f32 v154, v154\n"
            "v_mfma_f32_16x16x32_bf16 v[100:103], v[232:235], v[184:187], v[100:103]\n"
            "ds_read_b64_tr_b16 v[224:225], v20 offset:0\n"
            "ds_read_b64_tr_b16 v[226:227], v20 offset:2048\n"
            "ds_read_b64_tr_b16 v[228:229], v20 offset:4096\n"
            "ds_read_b64_tr_b16 v[230:231], v20 offset:6144\n"
            "v_exp_f32 v155, v155\n"
            "v_cvt_pk_bf16_f32 v180, v152, v153\n"
            "v_mfma_f32_16x16x32_bf16 v[104:107], v[240:243], v[184:187], v[104:107]\n"
            "v_exp_f32 v156, v156\n"
            "v_mfma_f32_16x16x32_bf16 v[104:107], v[244:247], v[188:191], v[104:107]\n"
            "ds_read_b64_tr_b16 v[232:233], v21 offset:0\n"
            "ds_read_b64_tr_b16 v[234:235], v21 offset:2048\n"
            "ds_read_b64_tr_b16 v[236:237], v21 offset:4096\n"
            "ds_read_b64_tr_b16 v[238:239], v21 offset:6144\n"
            "v_exp_f32 v157, v157\n"
            "v_cvt_pk_bf16_f32 v181, v154, v155\n"
            "v_mfma_f32_16x16x32_bf16 v[108:111], v[252:255], v[188:191], v[108:111]\n"
            "v_exp_f32 v158, v158\n"
            "v_mfma_f32_16x16x32_bf16 v[108:111], v[248:251], v[184:187], v[108:111]\n"
            "ds_read_b64_tr_b16 v[240:241], v22 offset:0\n"
            "ds_read_b64_tr_b16 v[242:243], v22 offset:2048\n"
            "ds_read_b64_tr_b16 v[244:245], v22 offset:4096\n"
            "ds_read_b64_tr_b16 v[246:247], v22 offset:6144\n"
            "v_exp_f32 v159, v159\n"
            "v_cvt_pk_bf16_f32 v182, v156, v157\n"
            "v_mfma_f32_16x16x32_bf16 v[144:147], v[192:195], v[40:43], 0\n"
            "v_exp_f32 v160, v160\n"
            "v_mfma_f32_16x16x32_bf16 v[144:147], v[196:199], v[44:47], v[144:147]\n"
            "ds_read_b64_tr_b16 v[248:249], v23 offset:0\n"
            "ds_read_b64_tr_b16 v[250:251], v23 offset:2048\n"
            "ds_read_b64_tr_b16 v[252:253], v23 offset:4096\n"
            "ds_read_b64_tr_b16 v[254:255], v23 offset:6144\n"
            "v_exp_f32 v161, v161\n"
            "v_cvt_pk_bf16_f32 v183, v158, v159\n"
            "v_mfma_f32_16x16x32_bf16 v[148:151], v[200:203], v[40:43], 0\n"
            "v_exp_f32 v162, v162\n"
            "v_mfma_f32_16x16x32_bf16 v[148:151], v[204:207], v[44:47], v[148:151]\n"
            "v_exp_f32 v163, v163\n"
            "v_cvt_pk_bf16_f32 v184, v160, v161\n"
            "v_mfma_f32_16x16x32_bf16 v[152:155], v[208:211], v[40:43], 0\n"
            "v_exp_f32 v164, v164\n"
            "v_mfma_f32_16x16x32_bf16 v[152:155], v[212:215], v[44:47], v[152:155]\n"
            "v_exp_f32 v165, v165\n"
            "v_cvt_pk_bf16_f32 v185, v162, v163\n"
            "v_mfma_f32_16x16x32_bf16 v[156:159], v[216:219], v[40:43], 0\n"
            "v_exp_f32 v166, v166\n"
            "v_mfma_f32_16x16x32_bf16 v[156:159], v[220:223], v[44:47], v[156:159]\n"
            "s_waitcnt lgkmcnt(12)\n"
            "v_exp_f32 v167, v167\n"
            "v_cvt_pk_bf16_f32 v186, v164, v165\n"
            "v_mfma_f32_16x16x32_bf16 v[64:67], v[224:227], v[176:179], v[64:67]\n"
            "v_exp_f32 v168, v168\n"
            "v_mfma_f32_16x16x32_bf16 v[64:67], v[228:231], v[180:183], v[64:67]\n"
            "s_waitcnt lgkmcnt(8)\n"
            "v_exp_f32 v169, v169\n"
            "v_cvt_pk_bf16_f32 v187, v166, v167\n"
            "v_mfma_f32_16x16x32_bf16 v[68:71], v[236:239], v[180:183], v[68:71]\n"
            "v_exp_f32 v170, v170\n"
            "v_mfma_f32_16x16x32_bf16 v[68:71], v[232:235], v[176:179], v[68:71]\n"
            "s_waitcnt lgkmcnt(4)\n"
            "v_exp_f32 v171, v171\n"
            "v_cvt_pk_bf16_f32 v188, v168, v169\n"
            "v_mfma_f32_16x16x32_bf16 v[72:75], v[240:243], v[176:179], v[72:75]\n"
            "v_exp_f32 v172, v172\n"
            "v_mfma_f32_16x16x32_bf16 v[72:75], v[244:247], v[180:183], v[72:75]\n"
            "s_waitcnt lgkmcnt(0)\n"
            "ds_write_b128 v19, v[64:67] offset:16384\n"
            "v_exp_f32 v173, v173\n"
            "v_cvt_pk_bf16_f32 v189, v170, v171\n"
            "v_mfma_f32_16x16x32_bf16 v[76:79], v[252:255], v[180:183], v[76:79]\n"
            "v_exp_f32 v174, v174\n"
            "v_mfma_f32_16x16x32_bf16 v[76:79], v[248:251], v[176:179], v[76:79]\n"
            "ds_write_b128 v19, v[68:71] offset:17408\n"
            "v_exp_f32 v175, v175\n"
            "v_cvt_pk_bf16_f32 v190, v172, v173\n"
            "v_mfma_f32_16x16x32_bf16 v[160:163], v[192:195], v[48:51], 0\n"
            "v_exp_f32 v144, v144\n"
            "v_mfma_f32_16x16x32_bf16 v[160:163], v[196:199], v[52:55], v[160:163]\n"
            "ds_write_b128 v19, v[72:75] offset:18432\n"
            "v_exp_f32 v145, v145\n"
            "v_cvt_pk_bf16_f32 v191, v174, v175\n"
            "v_mfma_f32_16x16x32_bf16 v[164:167], v[200:203], v[48:51], 0\n"
            "v_exp_f32 v146, v146\n"
            "v_mfma_f32_16x16x32_bf16 v[164:167], v[204:207], v[52:55], v[164:167]\n"
            "ds_write_b128 v19, v[76:79] offset:19456\n"
            "v_exp_f32 v147, v147\n"
            "v_cvt_pk_bf16_f32 v176, v144, v145\n"
            "v_mfma_f32_16x16x32_bf16 v[168:171], v[208:211], v[48:51], 0\n"
            "v_exp_f32 v148, v148\n"
            "v_mfma_f32_16x16x32_bf16 v[168:171], v[212:215], v[52:55], v[168:171]\n"
            "v_exp_f32 v149, v149\n"
            "v_cvt_pk_bf16_f32 v177, v146, v147\n"
            "v_mfma_f32_16x16x32_bf16 v[172:175], v[216:219], v[48:51], 0\n"
            "v_exp_f32 v150, v150\n"
            "v_mfma_f32_16x16x32_bf16 v[172:175], v[220:223], v[52:55], v[172:175]\n"
            "v_exp_f32 v151, v151\n"
            "v_cvt_pk_bf16_f32 v178, v148, v149\n"
            "v_mfma_f32_16x16x32_bf16 v[80:83], v[224:227], v[184:187], v[80:83]\n"
            "v_exp_f32 v152, v152\n"
            "v_mfma_f32_16x16x32_bf16 v[80:83], v[228:231], v[188:191], v[80:83]\n"
            "v_exp_f32 v153, v153\n"
            "v_cvt_pk_bf16_f32 v179, v150, v151\n"
            "v_mfma_f32_16x16x32_bf16 v[84:87], v[236:239], v[188:191], v[84:87]\n"
            "v_exp_f32 v154, v154\n"
            "v_mfma_f32_16x16x32_bf16 v[84:87], v[232:235], v[184:187], v[84:87]\n"
            "v_exp_f32 v155, v155\n"
            "v_cvt_pk_bf16_f32 v180, v152, v153\n"
            "v_mfma_f32_16x16x32_bf16 v[88:91], v[240:243], v[184:187], v[88:91]\n"
            "v_exp_f32 v156, v156\n"
            "v_mfma_f32_16x16x32_bf16 v[88:91], v[244:247], v[188:191], v[88:91]\n"
            "ds_write_b128 v19, v[80:83] offset:20480\n"
            "v_exp_f32 v157, v157\n"
            "v_cvt_pk_bf16_f32 v181, v154, v155\n"
            "v_mfma_f32_16x16x32_bf16 v[92:95], v[252:255], v[188:191], v[92:95]\n"
            "v_exp_f32 v158, v158\n"
            "v_mfma_f32_16x16x32_bf16 v[92:95], v[248:251], v[184:187], v[92:95]\n"
            "ds_write_b128 v19, v[84:87] offset:21504\n"
            "v_exp_f32 v159, v159\n"
            "v_cvt_pk_bf16_f32 v182, v156, v157\n"
            "v_mfma_f32_16x16x32_bf16 v[144:147], v[192:195], v[56:59], 0\n"
            "v_exp_f32 v160, v160\n"
            "v_mfma_f32_16x16x32_bf16 v[144:147], v[196:199], v[60:63], v[144:147]\n"
            "ds_write_b128 v19, v[88:91] offset:22528\n"
            "v_exp_f32 v161, v161\n"
            "v_cvt_pk_bf16_f32 v183, v158, v159\n"
            "v_mfma_f32_16x16x32_bf16 v[148:151], v[200:203], v[56:59], 0\n"
            "v_exp_f32 v162, v162\n"
            "v_mfma_f32_16x16x32_bf16 v[148:151], v[204:207], v[60:63], v[148:151]\n"
            "ds_read_b128 v[192:195], v16 offset:8192\n"
            "ds_read_b128 v[196:199], v17 offset:8192\n"
            "ds_write_b128 v19, v[92:95] offset:23552\n"
            "v_exp_f32 v163, v163\n"
            "v_cvt_pk_bf16_f32 v184, v160, v161\n"
            "v_mfma_f32_16x16x32_bf16 v[152:155], v[208:211], v[56:59], 0\n"
            "v_exp_f32 v164, v164\n"
            "v_mfma_f32_16x16x32_bf16 v[152:155], v[212:215], v[60:63], v[152:155]\n"
            "ds_read_b128 v[200:203], v16 offset:10240\n"
            "ds_read_b128 v[204:207], v17 offset:10240\n"
            "v_exp_f32 v165, v165\n"
            "v_cvt_pk_bf16_f32 v185, v162, v163\n"
            "v_mfma_f32_16x16x32_bf16 v[156:159], v[216:219], v[56:59], 0\n"
            "v_exp_f32 v166, v166\n"
            "v_mfma_f32_16x16x32_bf16 v[156:159], v[220:223], v[60:63], v[156:159]\n"
            "ds_read_b128 v[208:211], v16 offset:12288\n"
            "ds_read_b128 v[212:215], v17 offset:12288\n"
            "s_waitcnt lgkmcnt(4)\n"
            "s_barrier\n"
            "v_exp_f32 v167, v167\n"
            "v_cvt_pk_bf16_f32 v186, v164, v165\n"
            "v_mfma_f32_16x16x32_bf16 v[96:99], v[224:227], v[176:179], v[96:99]\n"
            "v_exp_f32 v168, v168\n"
            "v_mfma_f32_16x16x32_bf16 v[96:99], v[228:231], v[180:183], v[96:99]\n"
            "ds_read_b128 v[216:219], v16 offset:14336\n"
            "ds_read_b128 v[220:223], v17 offset:14336\n"
            "v_exp_f32 v169, v169\n"
            "v_cvt_pk_bf16_f32 v187, v166, v167\n"
            "v_mfma_f32_16x16x32_bf16 v[100:103], v[236:239], v[180:183], v[100:103]\n"
            "v_exp_f32 v170, v170\n"
            "v_mfma_f32_16x16x32_bf16 v[100:103], v[232:235], v[176:179], v[100:103]\n"
            "v_exp_f32 v171, v171\n"
            "v_cvt_pk_bf16_f32 v188, v168, v169\n"
            "v_mfma_f32_16x16x32_bf16 v[104:107], v[240:243], v[176:179], v[104:107]\n"
            "v_exp_f32 v172, v172\n"
            "v_mfma_f32_16x16x32_bf16 v[104:107], v[244:247], v[180:183], v[104:107]\n"
            "ds_write_b128 v19, v[96:99] offset:0\n"
            "v_exp_f32 v173, v173\n"
            "v_cvt_pk_bf16_f32 v189, v170, v171\n"
            "v_mfma_f32_16x16x32_bf16 v[108:111], v[252:255], v[180:183], v[108:111]\n"
            "v_exp_f32 v174, v174\n"
            "v_mfma_f32_16x16x32_bf16 v[108:111], v[248:251], v[176:179], v[108:111]\n"
            "s_waitcnt lgkmcnt(8)\n"
            "ds_write_b128 v19, v[100:103] offset:1024\n"
            "v_exp_f32 v175, v175\n"
            "v_cvt_pk_bf16_f32 v190, v172, v173\n"
            "v_mfma_f32_16x16x32_bf16 v[160:163], v[192:195], v[48:51], 0\n"
            "v_exp_f32 v144, v144\n"
            "v_mfma_f32_16x16x32_bf16 v[160:163], v[196:199], v[52:55], v[160:163]\n"
            "s_waitcnt lgkmcnt(6)\n"
            "ds_write_b128 v19, v[104:107] offset:2048\n"
            "v_exp_f32 v145, v145\n"
            "v_cvt_pk_bf16_f32 v191, v174, v175\n"
            "v_mfma_f32_16x16x32_bf16 v[164:167], v[200:203], v[48:51], 0\n"
            "v_exp_f32 v146, v146\n"
            "v_mfma_f32_16x16x32_bf16 v[164:167], v[204:207], v[52:55], v[164:167]\n"
            "s_waitcnt lgkmcnt(5)\n"
            "ds_write_b128 v19, v[108:111] offset:3072\n"
            "v_exp_f32 v147, v147\n"
            "v_cvt_pk_bf16_f32 v176, v144, v145\n"
            "v_mfma_f32_16x16x32_bf16 v[168:171], v[208:211], v[48:51], 0\n"
            "v_exp_f32 v148, v148\n"
            "v_mfma_f32_16x16x32_bf16 v[168:171], v[212:215], v[52:55], v[168:171]\n"
            "s_waitcnt lgkmcnt(4)\n"
            "v_exp_f32 v149, v149\n"
            "v_cvt_pk_bf16_f32 v177, v146, v147\n"
            "v_mfma_f32_16x16x32_bf16 v[172:175], v[216:219], v[48:51], 0\n"
            "v_exp_f32 v150, v150\n"
            "v_mfma_f32_16x16x32_bf16 v[172:175], v[220:223], v[52:55], v[172:175]\n"
            "s_waitcnt lgkmcnt(0)\n"
            "s_barrier\n"
            "v_exp_f32 v151, v151\n"
            "v_cvt_pk_bf16_f32 v178, v148, v149\n"
            "v_mfma_f32_16x16x32_bf16 v[112:115], v[224:227], v[184:187], v[112:115]\n"
            "v_exp_f32 v152, v152\n"
            "v_mfma_f32_16x16x32_bf16 v[112:115], v[228:231], v[188:191], v[112:115]\n"
            "v_exp_f32 v153, v153\n"
            "v_cvt_pk_bf16_f32 v179, v150, v151\n"
            "v_mfma_f32_16x16x32_bf16 v[116:119], v[236:239], v[188:191], v[116:119]\n"
            "v_exp_f32 v154, v154\n"
            "v_mfma_f32_16x16x32_bf16 v[116:119], v[232:235], v[184:187], v[116:119]\n"
            "v_exp_f32 v155, v155\n"
            "v_cvt_pk_bf16_f32 v180, v152, v153\n"
            "v_mfma_f32_16x16x32_bf16 v[120:123], v[240:243], v[184:187], v[120:123]\n"
            "v_exp_f32 v156, v156\n"
            "v_mfma_f32_16x16x32_bf16 v[120:123], v[244:247], v[188:191], v[120:123]\n"
            "v_exp_f32 v157, v157\n"
            "v_cvt_pk_bf16_f32 v181, v154, v155\n"
            "v_mfma_f32_16x16x32_bf16 v[124:127], v[252:255], v[188:191], v[124:127]\n"
            "v_exp_f32 v158, v158\n"
            "v_mfma_f32_16x16x32_bf16 v[124:127], v[248:251], v[184:187], v[124:127]\n"
            "v_exp_f32 v159, v159\n"
            "v_cvt_pk_bf16_f32 v182, v156, v157\n"
            "v_mfma_f32_16x16x32_bf16 v[144:147], v[192:195], v[56:59], 0\n"
            "v_exp_f32 v160, v160\n"
            "v_mfma_f32_16x16x32_bf16 v[144:147], v[196:199], v[60:63], v[144:147]\n"
            "v_exp_f32 v161, v161\n"
            "v_cvt_pk_bf16_f32 v183, v158, v159\n"
            "v_mfma_f32_16x16x32_bf16 v[148:151], v[200:203], v[56:59], 0\n"
            "v_exp_f32 v162, v162\n"
            "v_mfma_f32_16x16x32_bf16 v[148:151], v[204:207], v[60:63], v[148:151]\n"
            "v_exp_f32 v163, v163\n"
            "v_cvt_pk_bf16_f32 v184, v160, v161\n"
            "v_mfma_f32_16x16x32_bf16 v[152:155], v[208:211], v[56:59], 0\n"
            "v_exp_f32 v164, v164\n"
            "v_mfma_f32_16x16x32_bf16 v[152:155], v[212:215], v[60:63], v[152:155]\n"
            "v_exp_f32 v165, v165\n"
            "v_cvt_pk_bf16_f32 v185, v162, v163\n"
            "v_mfma_f32_16x16x32_bf16 v[156:159], v[216:219], v[56:59], 0\n"
            "v_exp_f32 v166, v166\n"
            "v_mfma_f32_16x16x32_bf16 v[156:159], v[220:223], v[60:63], v[156:159]\n"
            "v_exp_f32 v167, v167\n"
            "v_cvt_pk_bf16_f32 v186, v164, v165\n"
            "v_mfma_f32_16x16x32_bf16 v[128:131], v[224:227], v[176:179], v[128:131]\n"
            "v_exp_f32 v168, v168\n"
            "v_mfma_f32_16x16x32_bf16 v[128:131], v[228:231], v[180:183], v[128:131]\n"
            "v_exp_f32 v169, v169\n"
            "v_cvt_pk_bf16_f32 v187, v166, v167\n"
            "v_mfma_f32_16x16x32_bf16 v[132:135], v[236:239], v[180:183], v[132:135]\n"
            "v_exp_f32 v170, v170\n"
            "v_mfma_f32_16x16x32_bf16 v[132:135], v[232:235], v[176:179], v[132:135]\n"
            "ds_read_b64_tr_b16 v[224:225], v20 offset:8192\n"
            "ds_read_b64_tr_b16 v[226:227], v20 offset:10240\n"
            "ds_read_b64_tr_b16 v[228:229], v20 offset:12288\n"
            "ds_read_b64_tr_b16 v[230:231], v20 offset:14336\n"
            "v_exp_f32 v171, v171\n"
            "v_cvt_pk_bf16_f32 v188, v168, v169\n"
            "v_mfma_f32_16x16x32_bf16 v[136:139], v[240:243], v[176:179], v[136:139]\n"
            "v_exp_f32 v172, v172\n"
            "v_mfma_f32_16x16x32_bf16 v[136:139], v[244:247], v[180:183], v[136:139]\n"
            "ds_read_b64_tr_b16 v[232:233], v21 offset:8192\n"
            "ds_read_b64_tr_b16 v[234:235], v21 offset:10240\n"
            "ds_read_b64_tr_b16 v[236:237], v21 offset:12288\n"
            "ds_read_b64_tr_b16 v[238:239], v21 offset:14336\n"
            "v_exp_f32 v173, v173\n"
            "v_cvt_pk_bf16_f32 v189, v170, v171\n"
            "v_mfma_f32_16x16x32_bf16 v[140:143], v[252:255], v[180:183], v[140:143]\n"
            "v_exp_f32 v174, v174\n"
            "v_mfma_f32_16x16x32_bf16 v[140:143], v[248:251], v[176:179], v[140:143]\n"
            "ds_read_b64_tr_b16 v[240:241], v22 offset:8192\n"
            "ds_read_b64_tr_b16 v[242:243], v22 offset:10240\n"
            "ds_read_b64_tr_b16 v[244:245], v22 offset:12288\n"
            "ds_read_b64_tr_b16 v[246:247], v22 offset:14336\n"
            "v_exp_f32 v175, v175\n"
            "v_cvt_pk_bf16_f32 v190, v172, v173\n"
            "ds_read_b64_tr_b16 v[248:249], v23 offset:8192\n"
            "ds_read_b64_tr_b16 v[250:251], v23 offset:10240\n"
            "ds_read_b64_tr_b16 v[252:253], v23 offset:12288\n"
            "ds_read_b64_tr_b16 v[254:255], v23 offset:14336\n"
            "v_exp_f32 v144, v144\n"
            "v_cvt_pk_bf16_f32 v191, v174, v175\n"
            "v_exp_f32 v145, v145\n"
            "v_exp_f32 v146, v146\n"
            "v_exp_f32 v147, v147\n"
            "v_cvt_pk_bf16_f32 v176, v144, v145\n"
            "v_exp_f32 v148, v148\n"
            "v_exp_f32 v149, v149\n"
            "v_cvt_pk_bf16_f32 v177, v146, v147\n"
            "s_waitcnt lgkmcnt(12)\n"
            "v_mfma_f32_16x16x32_bf16 v[112:115], v[224:227], v[184:187], v[112:115]\n"
            "v_exp_f32 v150, v150\n"
            "v_mfma_f32_16x16x32_bf16 v[112:115], v[228:231], v[188:191], v[112:115]\n"
            "v_exp_f32 v151, v151\n"
            "v_exp_f32 v152, v152\n"
            "v_cvt_pk_bf16_f32 v178, v148, v149\n"
            "s_waitcnt lgkmcnt(8)\n"
            "v_mfma_f32_16x16x32_bf16 v[116:119], v[232:235], v[184:187], v[116:119]\n"
            "v_exp_f32 v153, v153\n"
            "v_mfma_f32_16x16x32_bf16 v[116:119], v[236:239], v[188:191], v[116:119]\n"
            "v_exp_f32 v154, v154\n"
            "v_cvt_pk_bf16_f32 v179, v150, v151\n"
            "s_waitcnt lgkmcnt(4)\n"
            "v_mfma_f32_16x16x32_bf16 v[120:123], v[240:243], v[184:187], v[120:123]\n"
            "v_exp_f32 v155, v155\n"
            "v_mfma_f32_16x16x32_bf16 v[120:123], v[244:247], v[188:191], v[120:123]\n"
            "ds_write_b128 v19, v[112:115] offset:4096\n"
            "v_exp_f32 v156, v156\n"
            "v_cvt_pk_bf16_f32 v180, v152, v153\n"
            "s_waitcnt lgkmcnt(1)\n"
            "v_mfma_f32_16x16x32_bf16 v[124:127], v[248:251], v[184:187], v[124:127]\n"
            "v_exp_f32 v157, v157\n"
            "v_mfma_f32_16x16x32_bf16 v[124:127], v[252:255], v[188:191], v[124:127]\n"
            "ds_write_b128 v19, v[116:119] offset:5120\n"
            "v_exp_f32 v158, v158\n"
            "v_cvt_pk_bf16_f32 v181, v154, v155\n"
            "v_mfma_f32_16x16x32_bf16 v[128:131], v[224:227], v[176:179], v[128:131]\n"
            "v_exp_f32 v159, v159\n"
            "v_cvt_pk_bf16_f32 v182, v156, v157\n"
            "v_mfma_f32_16x16x32_bf16 v[132:135], v[232:235], v[176:179], v[132:135]\n"
            "ds_write_b128 v19, v[120:123] offset:6144\n"
            "v_cvt_pk_bf16_f32 v183, v158, v159\n"
            "v_mfma_f32_16x16x32_bf16 v[136:139], v[240:243], v[176:179], v[136:139]\n"
            "s_nop 0\n"
            "v_mfma_f32_16x16x32_bf16 v[128:131], v[228:231], v[180:183], v[128:131]\n"
            "ds_write_b128 v19, v[124:127] offset:7168\n"
            "v_mfma_f32_16x16x32_bf16 v[132:135], v[236:239], v[180:183], v[132:135]\n"
            "v_mfma_f32_16x16x32_bf16 v[136:139], v[244:247], v[180:183], v[136:139]\n"
            "v_mfma_f32_16x16x32_bf16 v[140:143], v[248:251], v[176:179], v[140:143]\n"
            "v_mfma_f32_16x16x32_bf16 v[140:143], v[252:255], v[180:183], v[140:143]\n"
            "s_nop 2\n"
            "ds_write_b128 v19, v[128:131] offset:8192\n"
            "s_nop 0\n"
            "ds_write_b128 v19, v[132:135] offset:9216\n"
            "ds_write_b128 v19, v[136:139] offset:10240\n"
            "s_nop 1\n"
            "ds_write_b128 v19, v[140:143] offset:11264\n"
            "s_waitcnt lgkmcnt(0)\n"
            "s_barrier\n"
            "s_endpgm\n"
            "14:\n"
            "s_mov_b32 m0, 1048576\n"
            "s_waitcnt vmcnt(0)\n"
            "3:\n"
            "v_cmp_ne_u64 vcc, s[80:81], v[6:7]\n"
            "s_sub_u32 m0, m0, 1\n"
            "s_cbranch_scc1 15b\n"
            "s_nop 2\n"
            "s_cbranch_vccz 15b\n"
            "buffer_load_dwordx2 v[6:7], v4, s[40:43], 0 offen offset:768 sc1\n"
            "s_waitcnt vmcnt(0)\n"
            "s_branch 3b\n"
            "8:\n"
            "ds_write_b32 v160, v161 offset:248\n"
            "s_waitcnt vmcnt(0)\n"
            "s_branch 99f\n"
            "31:\n"
            "s_mul_i32 s65, %[me], 10240\n"
            "v_lshl_add_u32 v1, v0, 4, s65\n"
            "s_addk_i32 s65, 6144\n"
            "v_lshl_add_u32 v2, v0, 3, s65\n"
            "v_mov_b32 v3, 0\n"
            "s_waitcnt lgkmcnt(0)\n"
            "v_and_b32 v4, 255, v0\n"
            "v_mov_b64 v[16:17], -1\n"
            "v_mov_b64 v[18:19], -1\n"
            "v_lshlrev_b32 v5, 4, v4\n"
            "v_lshlrev_b32 v6, 2, v4\n"
            "v_add_u32 v7, v5, v6\n"
            "v_and_b32 v8, 255, v6\n"
            "v_mul_u32_u24 v9, 66, v8\n"
            "v_cmp_gt_u32 vcc, 160, v8\n"
            "ds_write_b128 v5, v[16:19] offset:256\n"
            "ds_write_b128 v5, v[16:19] offset:4352\n"
            "ds_write_b128 v5, v[16:19] offset:8448\n"
            "ds_write_b32 v6, v3 offset:31424\n"
            "v_add_u32 v9, 96, v9\n"
            "v_cndmask_b32 v9, 0, v9, vcc\n"
            "v_add_u32 v10, 10944, v7\n"
            "v_add_u32 v11, 16064, v7\n"
            "v_add_u32 v12, 21184, v7\n"
            "v_add_u32 v13, 26304, v7\n"
            "ds_write_b32 v8, v9\n"
            "ds_write2_b32 v10, v16, v16 offset1:1\n"
            "ds_write2_b32 v11, v16, v16 offset1:1\n"
            "ds_write2_b32 v12, v16, v16 offset1:1\n"
            "ds_write2_b32 v13, v16, v16 offset1:1\n"
            "s_waitcnt lgkmcnt(0)\n"
            "s_barrier\n"
            "global_load_dwordx4 v[160:163], v1, s[68:69] offset:-4096 nt\n"
            "global_load_dwordx4 v[164:167], v1, s[68:69] nt\n"
            "global_load_dwordx2 v[168:169], v2, s[68:69] nt\n"
            "global_load_dwordx4 v[170:173], v1, s[70:71] offset:-4096 nt\n"
            "global_load_dwordx4 v[174:177], v1, s[70:71] nt\n"
            "global_load_dwordx2 v[178:179], v2, s[70:71] nt\n"
            "global_load_dwordx2 v[180:181], v3, s[48:49] sc1\n"
            "99:\n"
            "s_waitcnt lgkmcnt(0)\n"
        : "=&{s[80:81]}"(anonce), "=&{v[160:175]}"(ed), "=&{v[176:183]}"(ee)
        : "{v0}"(threadIdx.x), [wave] "s"(wave), [bid] "s"(bid), [me] "s"(me), [karg] "s"(__builtin_amdgcn_kernarg_segment_ptr())
        : "scc", "vcc", "memory", "v1", "v2", "v3", "v4", "v5", "v6", "v7", "v8", "v9", "v10", "v11", "v12", "v13", "v14", "v16", "v17", "v18", "v19", "v20", "v21", "v22", "v23", "v24", "v25", "v26", "v27", "v28", "v29", "v30", "v31", "v32", "v33", "v34", "v35", "v36", "v37", "v38", "v39", "v40", "v41", "v42", "v43", "v44", "v45", "v46", "v47", "v48", "v49", "v50", "v51", "v52", "v53", "v54", "v55", "v56", "v57", "v58", "v59", "v60", "v61", "v62", "v63", "v64", "v65", "v66", "v67", "v68", "v69", "v70", "v71", "v72", "v73", "v74", "v75", "v76", "v77", "v78", "v79", "v80", "v81", "v82", "v83", "v84", "v85", "v86", "v87", "v88", "v89", "v90", "v91", "v92", "v93", "v94", "v95", "v96", "v97", "v98", "v99", "v100", "v101", "v102", "v103", "v104", "v105", "v106", "v107", "v108", "v109", "v110", "v111", "v112", "v113", "v114", "v115", "v116", "v117", "v118", "v119", "v120", "v121", "v122", "v123", "v124", "v125", "v126", "v127", "v128", "v129", "v130", "v131", "v132", "v133", "v134", "v135", "v136", "v137", "v138", "v139", "v140", "v141", "v142", "v143", "v144", "v145", "v146", "v147", "v148", "v149", "v150", "v151", "v152", "v153", "v154", "v155", "v156", "v157", "v158", "v159", "v184", "v185", "v186", "v187", "v188", "v189", "v190", "v191", "v192", "v193", "v194", "v195", "v196", "v197", "v198", "v199", "v200", "v201", "v202", "v203", "v204", "v205", "v206", "v207", "v208", "v209", "v210", "v211", "v212", "v213", "v214", "v215", "v216", "v217", "v218", "v219", "v220", "v221", "v222", "v223", "v224", "v225", "v226", "v227", "v228", "v229", "v230", "v231", "v232", "v233", "v234", "v235", "v236", "v237", "v238", "v239", "v240", "v241", "v242", "v243", "v244", "v245", "v246", "v247", "v248", "v249", "v250", "v251", "v252", "v253", "v254", "v255", "s40", "s41", "s42", "s43", "s44", "s45", "s46", "s47", "s48", "s49", "s50", "s51", "s52", "s53", "s54", "s55", "s56", "s57", "s58", "s59", "s60", "s61", "s62", "s63", "s64", "s65", "s66", "s67", "s68", "s69", "s70", "s71", "s82", "s83");
    __builtin_assume(wave >= 0 && wave < 8);
    const int half = me & 1, j = half * 4 + wave;
    const rsrc_t rs = __builtin_amdgcn_make_buffer_rsrc(ws, 0, WS_END, 0x00020000);
    if (__builtin_expect(me >= NWG, 0)) {
        if (me >= NWG + 2) return;
        const int lane = lane_id(), i = lane & 15, g = lane >> 4;
        if (wave >= 4) {
            if (wave > 4) return;
            const u32x2 nonce = make_nonce(ld64(rs, OFF_E));
            if (half) {
                wait_flag<4>(rs, OFF_FU + 64 * (NN / 16 - 1), nonce);
                const unsigned ub = OFF_U + (NN - 16) * 128;
                unsigned v[16];
#pragma unroll
                for (int k = 0; k < 16; ++k) v[k] = __builtin_amdgcn_raw_buffer_load_b16(rs, (4 * g + (k & 3)) * 128 + (16 * (k >> 2) + i) * 2, ub, 16);
#pragma unroll
                for (int k = 0; k < 2; ++k) {
                    st128(rs, OFF_W + (16 + k) * 1024 + lane * 16, ld128(rs, ub + i * 128 + 64 * k + 16 * g));
                    st128(rs, OFF_W + (18 + k) * 1024 + lane * 16, u32x4{v[8 * k] | v[8 * k + 1] << 16, v[8 * k + 2] | v[8 * k + 3] << 16, v[8 * k + 4] | v[8 * k + 5] << 16, v[8 * k + 6] | v[8 * k + 7] << 16});
                }
                DRAIN();
                if (lane == 0) st64(rs, OFF_FW + 64, nonce);
                return;
            }
            wait_flag<4>(rs, OFF_FW + 8 * min(lane, 8), nonce);
#pragma unroll 1
            for (int sp = 0; sp < SPIN; ++sp) {
                bool ok = true;
#pragma unroll
                for (int k = 0; k < 4; ++k) {
                    const u32x2 f = ld64(rs, OFF_FE + 64 * min(64 * k + lane, NWG - 1));
                    ok &= f[0] == nonce[0] && f[1] == nonce[1];
                }
                if (__all(ok)) break;
                asm volatile("s_sleep 2" ::: "memory");
            }
            asm volatile("" ::: "memory");
            if (lane <= (int)NREP) st64(rs, lane < (int)NREP ? OFF_ALL + 128 * lane : OFF_E, nonce);
            return;
        }
        const int m = half, eb = wave;
        const u16* w = m == 0 ? wv : wm;
        const u32x2 e2 = ld64(rs, OFF_E);
        u32x4 bf[2];
#pragma unroll
        for (int s = 0; s < 2; ++s)
#pragma unroll
            for (int j = 0; j < 4; ++j)
                bf[s][j] = (unsigned)wo[(32 * s + 8 * g + 2 * j) * 64 + perm(eb, i)] | ((unsigned)wo[(32 * s + 8 * g + 2 * j + 1) * 64 + perm(eb, i)] << 16);
        f32x4 t[4];
#pragma unroll
        for (int a = 0; a < 4; ++a)
            t[a] = mfma(*(const u32x4*)(w + (16 * a + i) * 64 + 32 + 8 * g), bf[1], mfma(*(const u32x4*)(w + (16 * a + i) * 64 + 8 * g), bf[0], f32x4{}));
        reflect(t, g);
        st128(rs, OFF_W + ((m * 4 + eb) * 2) * 1024 + lane * 16, pk8(t[0], t[1]));
        st128(rs, OFF_W + ((m * 4 + eb) * 2 + 1) * 1024 + lane * 16, pk8(t[2], t[3]));
        const u32x2 nonce = make_nonce(e2);
        DRAIN();
        if (lane == 0) st64(rs, OFF_FW + 8 * (m * 4 + eb), nonce);
        return;
    }
    if (__builtin_expect(wave < 4, 0)) {
        const u32x2 nonce = {(unsigned)__builtin_amdgcn_readfirstlane(anonce[0]), (unsigned)__builtin_amdgcn_readfirstlane(anonce[1])};
        attn_true_max(ws, rs, nonce, lds, L_RING + wave * RINGB, j * 1280, min(j * 1280 + 1280, NN));
        BAR(); BAR(); BAR();
        return;
    }
    const int lane = lane_id(), i = lane & 15, g = lane >> 4;
    const int hw = wave - 4, hid = hw * 64 + lane;
    __builtin_amdgcn_s_setprio(2);
    const size_t wb = (size_t)ws, ub = (size_t)(ws + OFF_U);
    const u32x4 srw = {(unsigned)wb, (unsigned)(wb >> 32) & 0xffffu, WS_END, 0x20000u};
    unsigned po = hid * 16, lane16 = lane * 16;
    const int lr = 16 * hw + i - 8 * half, lv = (unsigned)lr < (unsigned)NPB ? lr : 0, slot = half ? 2 + hw : 4 - hw;
    unsigned xo = (me * NPB + lv) * 128 + 16 * g, zo = L_ZL + lv * ZROW + 16 * g, pa = L_RING + hw * 1024 + lane16, po2 = L_RING + (slot - 2) * 4096 + lane16,
             qa = L_Q + slot * 2048 + lane16, i4 = 4 * i, g3 = g == 3 ? 0x3f800000u : 0u;
    asm volatile("" : "+v"(xo), "+v"(zo), "+v"(pa), "+v"(po2), "+v"(qa), "+v"(i4), "+v"(g3));
    u32x2 nv;
    {
        const unsigned one = (i & 3) == 3 ? 0x3f800000u : 0x3f80u, sa = i < 4 ? one : 0u, sb = (i >> 2) == 1 ? one : 0u;
        u32x4 sel = {sa, sb, sa, sb};
        unsigned sidx = (((i & 3) == 3 ? 0u : (i & 3)) | 12u) * 0x1111u;
        const u32x4 sru = {(unsigned)ub, ((unsigned)(ub >> 32) & 0xffffu) | (128u << 16), NN, 0x20000u};
        unsigned la = L_LIST + (hw * 10 + (i >> 3)) * LROW + 2 * g;
        unsigned za = L_ZL + (hw * 10 + (i >> 3)) * ZROW + (i & 7) * 32 + (g & 1) * 16;
        const unsigned cn = lane < 48 ? min(lane, NPB - 1) : min(hw * 10 + lane - 48, NPB - 1);
        unsigned beg = L_LIST - CBIAS + cn * LROW, ca = 4 * cn;
        asm volatile("" : "+v"(la), "+v"(za), "+v"(beg), "+v"(sel), "+v"(sidx), "+v"(ca), "+v"(lane16));
        u32x16 g0, g1, g2, g3; u32x4 mk; unsigned ovf, big;
        asm volatile(
            "v_mov_b64 v[128:129], -1\n"
            "v_mov_b64 v[130:131], -1\n"
            "v_mov_b64 v[104:105], 0\n"
            "v_add_u32 v241, 5120, %[ia]\n"
            "v_add_u32 v242, 10240, %[ia]\n"
            "v_add_u32 v243, 15360, %[ia]\n"
            "v_mov_b32 v65, %[co]\n"
            "v_mov_b32 v67, %[co]\n"
            "v_mov_b32 v69, %[co]\n"
            "v_mov_b32 v71, %[co]\n"
            "v_mov_b32 v73, %[co]\n"
            "v_mov_b32 v75, %[co]\n"
            "v_mov_b32 v77, %[co]\n"
            "v_mov_b32 v79, %[co]\n"
            "v_mov_b32 v81, %[co]\n"
            "v_mov_b32 v83, %[co]\n"
            "v_mov_b32 v85, %[co]\n"
            "v_mov_b32 v87, %[co]\n"
            "v_mov_b32 v89, %[co]\n"
            "v_mov_b32 v91, %[co]\n"
            "v_mov_b32 v93, %[co]\n"
            "v_mov_b32 v95, %[co]\n"
            "v_mov_b32 v97, %[co]\n"
            "v_mov_b32 v99, %[co]\n"
            "v_mov_b32 v101, %[co]\n"
            "v_mov_b32 v103, %[co]\n"
            "v_mov_b64 v[106:107], 0\n"
            "v_mov_b64 v[108:109], 0\n"
            "v_mov_b64 v[110:111], 0\n"
            "v_mov_b64 v[112:113], 0\n"
            "v_mov_b64 v[114:115], 0\n"
            "v_mov_b64 v[116:117], 0\n"
            "v_mov_b64 v[118:119], 0\n"
            "v_mov_b64 v[120:121], 0\n"
            "v_mov_b64 v[122:123], 0\n"
            "v_mov_b32 v124, 2\n"
            "v_mov_b32 v125, 1\n"
            "v_mov_b32 v126, 236\n"
            "v_mov_b64 v[140:141], -1\n"
            "v_add_u32 v60, 4096, %[po]\n"
            "v_add_u32 v61, 8192, %[po]\n"
            "v_add_u32 v62, 12288, %[po]\n"
            "s_mov_b32 s60, 52429\n"
            "s_waitcnt vmcnt(6)\n"
            "v_mul_u32_u24 v184, s60, v160\n"
            "v_lshrrev_b32 v184, 19, v184\n"
            "v_and_b32 v184, -4, v184\n"
            "ds_add_rtn_u32 v194, v184, v125 offset:31424\n"
            "v_mul_u32_u24 v185, s60, v161\n"
            "v_lshrrev_b32 v185, 19, v185\n"
            "v_and_b32 v185, -4, v185\n"
            "ds_add_rtn_u32 v195, v185, v125 offset:31424\n"
            "v_mul_u32_u24 v186, s60, v162\n"
            "v_lshrrev_b32 v186, 19, v186\n"
            "v_and_b32 v186, -4, v186\n"
            "ds_add_rtn_u32 v196, v186, v125 offset:31424\n"
            "v_mul_u32_u24 v187, s60, v163\n"
            "v_lshrrev_b32 v187, 19, v187\n"
            "v_and_b32 v187, -4, v187\n"
            "ds_add_rtn_u32 v197, v187, v125 offset:31424\n"
            "s_waitcnt vmcnt(5)\n"
            "v_mul_u32_u24 v188, s60, v164\n"
            "v_lshrrev_b32 v188, 19, v188\n"
            "v_and_b32 v188, -4, v188\n"
            "ds_add_rtn_u32 v198, v188, v125 offset:31424\n"
            "v_mul_u32_u24 v189, s60, v165\n"
            "v_lshrrev_b32 v189, 19, v189\n"
            "v_and_b32 v189, -4, v189\n"
            "ds_add_rtn_u32 v199, v189, v125 offset:31424\n"
            "v_mul_u32_u24 v190, s60, v166\n"
            "v_lshrrev_b32 v190, 19, v190\n"
            "v_and_b32 v190, -4, v190\n"
            "ds_add_rtn_u32 v200, v190, v125 offset:31424\n"
            "v_mul_u32_u24 v191, s60, v167\n"
            "v_lshrrev_b32 v191, 19, v191\n"
            "v_and_b32 v191, -4, v191\n"
            "ds_add_rtn_u32 v201, v191, v125 offset:31424\n"
            "s_waitcnt vmcnt(4)\n"
            "v_mul_u32_u24 v192, s60, v168\n"
            "v_lshrrev_b32 v192, 19, v192\n"
            "v_and_b32 v192, -4, v192\n"
            "ds_add_rtn_u32 v202, v192, v125 offset:31424\n"
            "v_mul_u32_u24 v193, s60, v169\n"
            "v_lshrrev_b32 v193, 19, v193\n"
            "v_and_b32 v193, -4, v193\n"
            "ds_add_rtn_u32 v203, v193, v125 offset:31424\n"
            "v_mad_i32_i24 v204, v184, -10, v160\n"
            "v_mad_i32_i24 v205, v185, -10, v161\n"
            "v_mad_i32_i24 v206, v186, -10, v162\n"
            "v_mad_i32_i24 v207, v187, -10, v163\n"
            "v_mad_i32_i24 v208, v188, -10, v164\n"
            "v_mad_i32_i24 v209, v189, -10, v165\n"
            "v_mad_i32_i24 v210, v190, -10, v166\n"
            "v_mad_i32_i24 v211, v191, -10, v167\n"
            "v_mad_i32_i24 v212, v192, -10, v168\n"
            "v_mad_i32_i24 v213, v193, -10, v169\n"
            "s_waitcnt vmcnt(0) lgkmcnt(0)\n"
            "v_add_u32 v58, 1, v180\n"
            "v_mov_b32 v59, v181\n"
            "v_readfirstlane_b32 s81, v181\n"
            "v_readfirstlane_b32 s80, v58\n"
            "v_min_u32 v194, 20, v194\n"
            "v_and_or_b32 v214, v194, 3, v184\n"
            "v_lshrrev_b32 v215, 2, v194\n"
            "v_mad_u32_u24 v214, v214, 20, v215\n"
            "v_add_u32 v215, v214, v215\n"
            "ds_write_b8 v214, v204 offset:10944\n"
            "ds_write_b16 v215, v170 offset:10950\n"
            "v_min_u32 v195, 20, v195\n"
            "v_and_or_b32 v217, v195, 3, v185\n"
            "v_lshrrev_b32 v218, 2, v195\n"
            "v_mad_u32_u24 v217, v217, 20, v218\n"
            "v_add_u32 v218, v217, v218\n"
            "ds_write_b8 v217, v205 offset:10944\n"
            "ds_write_b16 v218, v171 offset:10950\n"
            "v_min_u32 v196, 20, v196\n"
            "v_and_or_b32 v214, v196, 3, v186\n"
            "v_lshrrev_b32 v215, 2, v196\n"
            "v_mad_u32_u24 v214, v214, 20, v215\n"
            "v_add_u32 v215, v214, v215\n"
            "ds_write_b8 v214, v206 offset:10944\n"
            "ds_write_b16 v215, v172 offset:10950\n"
            "v_min_u32 v197, 20, v197\n"
            "v_and_or_b32 v217, v197, 3, v187\n"
            "v_lshrrev_b32 v218, 2, v197\n"
            "v_mad_u32_u24 v217, v217, 20, v218\n"
            "v_add_u32 v218, v217, v218\n"
            "ds_write_b8 v217, v207 offset:10944\n"
            "ds_write_b16 v218, v173 offset:10950\n"
            "v_min_u32 v198, 20, v198\n"
            "v_and_or_b32 v214, v198, 3, v188\n"
            "v_lshrrev_b32 v215, 2, v198\n"
            "v_mad_u32_u24 v214, v214, 20, v215\n"
            "v_add_u32 v215, v214, v215\n"
            "ds_write_b8 v214, v208 offset:10944\n"
            "ds_write_b16 v215, v174 offset:10950\n"
            "v_min_u32 v199, 20, v199\n"
            "v_and_or_b32 v217, v199, 3, v189\n"
            "v_lshrrev_b32 v218, 2, v199\n"
            "v_mad_u32_u24 v217, v217, 20, v218\n"
            "v_add_u32 v218, v217, v218\n"
            "ds_write_b8 v217, v209 offset:10944\n"
            "ds_write_b16 v218, v175 offset:10950\n"
            "v_min_u32 v200, 20, v200\n"
            "v_and_or_b32 v214, v200, 3, v190\n"
            "v_lshrrev_b32 v215, 2, v200\n"
            "v_mad_u32_u24 v214, v214, 20, v215\n"
            "v_add_u32 v215, v214, v215\n"
            "ds_write_b8 v214, v210 offset:10944\n"
            "ds_write_b16 v215, v176 offset:10950\n"
            "v_min_u32 v201, 20, v201\n"
            "v_and_or_b32 v217, v201, 3, v191\n"
            "v_lshrrev_b32 v218, 2, v201\n"
            "v_mad_u32_u24 v217, v217, 20, v218\n"
            "v_add_u32 v218, v217, v218\n"
            "ds_write_b8 v217, v211 offset:10944\n"
            "ds_write_b16 v218, v177 offset:10950\n"
            "v_min_u32 v202, 20, v202\n"
            "v_and_or_b32 v214, v202, 3, v192\n"
            "v_lshrrev_b32 v215, 2, v202\n"
            "v_mad_u32_u24 v214, v214, 20, v215\n"
            "v_add_u32 v215, v214, v215\n"
            "ds_write_b8 v214, v212 offset:10944\n"
            "ds_write_b16 v215, v178 offset:10950\n"
            "v_min_u32 v203, 20, v203\n"
            "v_and_or_b32 v217, v203, 3, v193\n"
            "v_lshrrev_b32 v218, 2, v203\n"
            "v_mad_u32_u24 v217, v217, 20, v218\n"
            "v_add_u32 v218, v217, v218\n"
            "ds_write_b8 v217, v213 offset:10944\n"
            "ds_write_b16 v218, v179 offset:10950\n"
            "v_max3_u32 v214, v194, v195, v196\n"
            "v_max3_u32 v214, v214, v197, v198\n"
            "v_max3_u32 v214, v214, v199, v200\n"
            "v_max3_u32 v214, v214, v201, v202\n"
            "v_max_u32 v214, v214, v203\n"
            "v_cmp_eq_u32 vcc, 20, v214\n"
            "v_mov_b32 v218, 164\n"
            "s_nop 3\n"
            "s_cbranch_vccz 11f\n"
            "v_cmp_eq_u32 vcc, 20, v194\n"
            "s_mov_b32 s60, 0\n"
            "s_nop 3\n"
            "s_cbranch_vccz 40f\n"
            "s_call_b64 s[62:63], 10f\n"
            "40:\n"
            "v_cmp_eq_u32 vcc, 20, v195\n"
            "s_mov_b32 s60, 1\n"
            "s_nop 3\n"
            "s_cbranch_vccz 41f\n"
            "s_call_b64 s[62:63], 10f\n"
            "41:\n"
            "v_cmp_eq_u32 vcc, 20, v196\n"
            "s_mov_b32 s60, 2\n"
            "s_nop 3\n"
            "s_cbranch_vccz 42f\n"
            "s_call_b64 s[62:63], 10f\n"
            "42:\n"
            "v_cmp_eq_u32 vcc, 20, v197\n"
            "s_mov_b32 s60, 3\n"
            "s_nop 3\n"
            "s_cbranch_vccz 43f\n"
            "s_call_b64 s[62:63], 10f\n"
            "43:\n"
            "v_cmp_eq_u32 vcc, 20, v198\n"
            "s_mov_b32 s60, 4\n"
            "s_nop 3\n"
            "s_cbranch_vccz 44f\n"
            "s_call_b64 s[62:63], 10f\n"
            "44:\n"
            "v_cmp_eq_u32 vcc, 20, v199\n"
            "s_mov_b32 s60, 5\n"
            "s_nop 3\n"
            "s_cbranch_vccz 45f\n"
            "s_call_b64 s[62:63], 10f\n"
            "45:\n"
            "v_cmp_eq_u32 vcc, 20, v200\n"
            "s_mov_b32 s60, 6\n"
            "s_nop 3\n"
            "s_cbranch_vccz 46f\n"
            "s_call_b64 s[62:63], 10f\n"
            "46:\n"
            "v_cmp_eq_u32 vcc, 20, v201\n"
            "s_mov_b32 s60, 7\n"
            "s_nop 3\n"
            "s_cbranch_vccz 47f\n"
            "s_call_b64 s[62:63], 10f\n"
            "47:\n"
            "v_cmp_eq_u32 vcc, 20, v202\n"
            "s_mov_b32 s60, 8\n"
            "s_nop 3\n"
            "s_cbranch_vccz 48f\n"
            "s_call_b64 s[62:63], 10f\n"
            "48:\n"
            "v_cmp_eq_u32 vcc, 20, v203\n"
            "s_mov_b32 s60, 9\n"
            "s_nop 3\n"
            "s_cbranch_vccz 49f\n"
            "s_call_b64 s[62:63], 10f\n"
            "49:\n"
            "s_branch 11f\n"
            "10:\n"
            "s_set_gpr_idx_on s60, gpr_idx(SRC0)\n"
            "v_mov_b32 v215, v170\n"
            "v_mov_b32 v216, v204\n"
            "v_mov_b32 v217, v184\n"
            "s_set_gpr_idx_off\n"
            "s_mov_b64 exec, vcc\n"
            "ds_add_rtn_u32 v214, v218, v125\n"
            "v_add_u32 v217, 4, v217\n"
            "v_lshl_or_b32 v215, v216, 16, v215\n"
            "v_lshl_or_b32 v215, v217, 22, v215\n"
            "s_waitcnt lgkmcnt(0)\n"
            "v_lshlrev_b32 v214, 2, v214\n"
            "v_min_u32 v216, 60, v214\n"
            "buffer_store_dword v215, v214, %[srw], %[oo] offen offset:4 sc1\n"
            "ds_write_b32 v216, v215 offset:168\n"
            "s_mov_b64 exec, -1\n"
            "s_setpc_b64 s[62:63]\n"
            "11:\n"
            "s_mov_b64 exec, 1\n"
            "ds_add_u32 v126, v125 offset:4\n"
            "s_mov_b64 exec, -1\n"
            "13:\n"
            "ds_read_b32 v214, v126 offset:4\n"
            "s_waitcnt lgkmcnt(0)\n"
            "v_cmp_gt_u32 vcc, 4, v214\n"
            "s_nop 4\n"
            "s_cbranch_vccnz 13b\n"
            "ds_read2_b32 v[224:225], %[ia] offset1:1\n"
            "ds_read2_b32 v[226:227], %[ia] offset0:2 offset1:3\n"
            "ds_read2_b32 v[228:229], v241 offset1:1\n"
            "ds_read2_b32 v[230:231], v241 offset0:2 offset1:3\n"
            "ds_read2_b32 v[232:233], v242 offset1:1\n"
            "ds_read2_b32 v[234:235], v242 offset0:2 offset1:3\n"
            "ds_read2_b32 v[236:237], v243 offset1:1\n"
            "ds_read2_b32 v[238:239], v243 offset0:2 offset1:3\n"
            "ds_read_b32 v240, %[i4] offset:164\n"
            "4:\n"
            "ds_read_b32 v144, v126\n"
            "s_waitcnt lgkmcnt(0)\n"
            "v_cmp_le_u32 vcc, 4, v144\n"
            "s_nop 4\n"
            "s_cbranch_vccnz 5f\n"
            "s_sleep 2\n"
            "s_branch 4b\n"
            "5:\n"
            "s_setprio 0\n"
            "s_mov_b32 s60, 0\n"
            "buffer_store_dwordx4 v[224:227], %[o0], %[srw], s60 offen sc1\n"
            "s_sleep 8\n"
            "s_mov_b32 s60, 1024000\n"
            "buffer_store_dwordx4 v[228:231], %[o0], %[srw], s60 offen sc1\n"
            "s_sleep 8\n"
            "s_mov_b32 s60, 2048000\n"
            "buffer_store_dwordx4 v[232:235], %[o0], %[srw], s60 offen sc1\n"
            "s_sleep 8\n"
            "s_mov_b32 s60, 3072000\n"
            "s_mov_b64 exec, %[m3]\n"
            "buffer_store_dwordx4 v[236:239], %[o0], %[srw], s60 offen sc1\n"
            "s_mov_b64 exec, 0xffff\n"
            "buffer_store_dword v240, %[i4], %[srw], %[oo] offen sc1\n"
            "s_mov_b64 exec, 1\n"
            "s_waitcnt vmcnt(0)\n"
            "ds_add_rtn_u32 v144, v126, v125 offset:4\n"
            "s_waitcnt lgkmcnt(0)\n"
            "v_cmp_eq_u32 vcc, 7, v144\n"
            "s_nop 4\n"
            "s_cbranch_vccz 7f\n"
            "buffer_store_dwordx2 v[58:59], off, %[srw], %[fe] sc1\n"
            "7:\n"
            "s_mov_b64 exec, -1\n"
            "s_mov_b32 s60, 1048576\n"
            "s_sleep 8\n"
            "buffer_store_dwordx4 v[128:131], %[po], %[srw], %[bo] offen sc1\n"
            "s_sleep 8\n"
            "s_mov_b64 exec, %[mb]\n"
            "s_cbranch_execz 6f\n"
            "buffer_store_dwordx4 v[128:131], v60, %[srw], %[bo] offen sc1\n"
            "6:\n"
            "s_mov_b64 exec, -1\n"
            "1:\n"
            "s_sleep 8\n"
            "buffer_load_dwordx2 v[144:145], off, %[srw], %[ao] sc1\n"
            "s_waitcnt vmcnt(0)\n"
            "v_cmp_ne_u64 vcc, s[80:81], v[144:145]\n"
            "s_sub_u32 s60, s60, 1\n"
            "s_cbranch_scc1 2f\n"
            "s_nop 2\n"
            "s_cbranch_vccnz 1b\n"
            "2:\n"
            "s_setprio 0\n"
            "buffer_load_dwordx4 v[128:131], %[po], %[srw], %[so] offen sc1\n"
            "buffer_load_dwordx4 v[132:135], v60, %[srw], %[so] offen sc1\n"
            "buffer_load_dwordx4 v[136:139], v61, %[srw], %[so] offen sc1\n"
            "s_mov_b64 exec, %[m3]\n"
            "buffer_load_dwordx4 v[140:143], v62, %[srw], %[so] offen sc1\n"
            "s_mov_b64 exec, -1\n"
            "s_mov_b32 %[ovf], 0\n"
            "s_waitcnt vmcnt(0)\n"
            "v_and_b32 v60, v129, v133\n"
            "v_and_b32 v61, v137, v141\n"
            "v_and_b32 v60, v60, v61\n"
            "v_cmp_le_i16 vcc, 0, v60\n"
            "s_mov_b64 s[62:63], 0\n"
            "s_nop 3\n"
            "s_cbranch_vccz 8f\n"
            "s_mov_b64 s[64:65], 0xffff\n"
            "v_cmp_le_i16 vcc, 0, v129\n"
            "v_mov_b32 v60, v129\n"
            "s_nop 3\n"
            "50:\n"
            "s_cbranch_vccz 60f\n"
            "s_mov_b64 exec, s[64:65]\n"
            "s_cbranch_execz 9f\n"
            "s_ff1_i32_b64 s60, vcc\n"
            "s_bitset0_b64 vcc, s60\n"
            "s_mul_i32 s60, s60, 2592\n"
            "s_add_u32 s60, s60, 0\n"
            "v_add_u32 v184, s60, %[i4]\n"
            "s_or_b64 s[62:63], s[62:63], exec\n"
            "s_lshl_b64 s[64:65], exec, 16\n"
            "s_mov_b64 exec, -1\n"
            "s_mov_b64 vcc, vcc\n"
            "s_branch 50b\n"
            "60:\n"
            "v_cmp_le_i16 vcc, 0, v133\n"
            "v_mov_b32 v61, v133\n"
            "s_nop 3\n"
            "51:\n"
            "s_cbranch_vccz 61f\n"
            "s_mov_b64 exec, s[64:65]\n"
            "s_cbranch_execz 9f\n"
            "s_ff1_i32_b64 s60, vcc\n"
            "s_bitset0_b64 vcc, s60\n"
            "s_mul_i32 s60, s60, 2592\n"
            "s_add_u32 s60, s60, 663552\n"
            "v_add_u32 v184, s60, %[i4]\n"
            "s_or_b64 s[62:63], s[62:63], exec\n"
            "s_lshl_b64 s[64:65], exec, 16\n"
            "s_mov_b64 exec, -1\n"
            "s_mov_b64 vcc, vcc\n"
            "s_branch 51b\n"
            "61:\n"
            "v_cmp_le_i16 vcc, 0, v137\n"
            "v_mov_b32 v62, v137\n"
            "s_nop 3\n"
            "52:\n"
            "s_cbranch_vccz 62f\n"
            "s_mov_b64 exec, s[64:65]\n"
            "s_cbranch_execz 9f\n"
            "s_ff1_i32_b64 s60, vcc\n"
            "s_bitset0_b64 vcc, s60\n"
            "s_mul_i32 s60, s60, 2592\n"
            "s_add_u32 s60, s60, 1327104\n"
            "v_add_u32 v184, s60, %[i4]\n"
            "s_or_b64 s[62:63], s[62:63], exec\n"
            "s_lshl_b64 s[64:65], exec, 16\n"
            "s_mov_b64 exec, -1\n"
            "s_mov_b64 vcc, vcc\n"
            "s_branch 52b\n"
            "62:\n"
            "v_cmp_le_i16 vcc, 0, v141\n"
            "v_mov_b32 v63, v141\n"
            "s_nop 3\n"
            "53:\n"
            "s_cbranch_vccz 63f\n"
            "s_mov_b64 exec, s[64:65]\n"
            "s_cbranch_execz 9f\n"
            "s_ff1_i32_b64 s60, vcc\n"
            "s_bitset0_b64 vcc, s60\n"
            "s_mul_i32 s60, s60, 2592\n"
            "s_add_u32 s60, s60, 1990656\n"
            "v_add_u32 v184, s60, %[i4]\n"
            "s_or_b64 s[62:63], s[62:63], exec\n"
            "s_lshl_b64 s[64:65], exec, 16\n"
            "s_mov_b64 exec, -1\n"
            "s_mov_b64 vcc, vcc\n"
            "s_branch 53b\n"
            "63:\n"
            "s_mov_b64 exec, s[62:63]\n"
            "buffer_load_dword v185, v184, %[srw], %[ob] offen sc1\n"
            "s_branch 14f\n"
            "9:\n"
            "s_mov_b64 exec, -1\n"
            "v_mov_b32 v60, v129\n"
            "v_mov_b32 v61, v133\n"
            "v_mov_b32 v62, v137\n"
            "v_mov_b32 v63, v141\n"
            "s_mov_b32 %[ovf], 1\n"
            "s_mov_b64 s[62:63], 0\n"
            "14:\n"
            "s_mov_b64 exec, -1\n"
            "8:\n"
            "v_lshlrev_b32_sdwa v144, v124, sext(v128) dst_sel:DWORD dst_unused:UNUSED_PAD src0_sel:DWORD src1_sel:BYTE_0\n"
            "v_lshlrev_b32_sdwa v145, v124, sext(v128) dst_sel:DWORD dst_unused:UNUSED_PAD src0_sel:DWORD src1_sel:BYTE_1\n"
            "v_lshlrev_b32_sdwa v146, v124, sext(v128) dst_sel:DWORD dst_unused:UNUSED_PAD src0_sel:DWORD src1_sel:BYTE_2\n"
            "v_lshlrev_b32_sdwa v147, v124, sext(v128) dst_sel:DWORD dst_unused:UNUSED_PAD src0_sel:DWORD src1_sel:BYTE_3\n"
            "v_lshlrev_b32_sdwa v148, v124, sext(v129) dst_sel:DWORD dst_unused:UNUSED_PAD src0_sel:DWORD src1_sel:BYTE_0\n"
            "ds_add_rtn_u32 v144, v144, v124\n"
            "ds_add_rtn_u32 v145, v145, v124\n"
            "ds_add_rtn_u32 v146, v146, v124\n"
            "ds_add_rtn_u32 v147, v147, v124\n"
            "ds_add_rtn_u32 v148, v148, v124\n"
            "v_lshlrev_b32_sdwa v149, v124, sext(v132) dst_sel:DWORD dst_unused:UNUSED_PAD src0_sel:DWORD src1_sel:BYTE_0\n"
            "v_lshlrev_b32_sdwa v150, v124, sext(v132) dst_sel:DWORD dst_unused:UNUSED_PAD src0_sel:DWORD src1_sel:BYTE_1\n"
            "v_lshlrev_b32_sdwa v151, v124, sext(v132) dst_sel:DWORD dst_unused:UNUSED_PAD src0_sel:DWORD src1_sel:BYTE_2\n"
            "v_lshlrev_b32_sdwa v152, v124, sext(v132) dst_sel:DWORD dst_unused:UNUSED_PAD src0_sel:DWORD src1_sel:BYTE_3\n"
            "v_lshlrev_b32_sdwa v153, v124, sext(v133) dst_sel:DWORD dst_unused:UNUSED_PAD src0_sel:DWORD src1_sel:BYTE_0\n"
            "ds_add_rtn_u32 v149, v149, v124\n"
            "ds_add_rtn_u32 v150, v150, v124\n"
            "ds_add_rtn_u32 v151, v151, v124\n"
            "ds_add_rtn_u32 v152, v152, v124\n"
            "ds_add_rtn_u32 v153, v153, v124\n"
            "v_lshlrev_b32_sdwa v154, v124, sext(v136) dst_sel:DWORD dst_unused:UNUSED_PAD src0_sel:DWORD src1_sel:BYTE_0\n"
            "v_lshlrev_b32_sdwa v155, v124, sext(v136) dst_sel:DWORD dst_unused:UNUSED_PAD src0_sel:DWORD src1_sel:BYTE_1\n"
            "v_lshlrev_b32_sdwa v156, v124, sext(v136) dst_sel:DWORD dst_unused:UNUSED_PAD src0_sel:DWORD src1_sel:BYTE_2\n"
            "v_lshlrev_b32_sdwa v157, v124, sext(v136) dst_sel:DWORD dst_unused:UNUSED_PAD src0_sel:DWORD src1_sel:BYTE_3\n"
            "v_lshlrev_b32_sdwa v158, v124, sext(v137) dst_sel:DWORD dst_unused:UNUSED_PAD src0_sel:DWORD src1_sel:BYTE_0\n"
            "ds_add_rtn_u32 v154, v154, v124\n"
            "ds_add_rtn_u32 v155, v155, v124\n"
            "ds_add_rtn_u32 v156, v156, v124\n"
            "ds_add_rtn_u32 v157, v157, v124\n"
            "ds_add_rtn_u32 v158, v158, v124\n"
            "v_lshlrev_b32_sdwa v159, v124, sext(v140) dst_sel:DWORD dst_unused:UNUSED_PAD src0_sel:DWORD src1_sel:BYTE_0\n"
            "v_lshlrev_b32_sdwa v160, v124, sext(v140) dst_sel:DWORD dst_unused:UNUSED_PAD src0_sel:DWORD src1_sel:BYTE_1\n"
            "v_lshlrev_b32_sdwa v161, v124, sext(v140) dst_sel:DWORD dst_unused:UNUSED_PAD src0_sel:DWORD src1_sel:BYTE_2\n"
            "v_lshlrev_b32_sdwa v162, v124, sext(v140) dst_sel:DWORD dst_unused:UNUSED_PAD src0_sel:DWORD src1_sel:BYTE_3\n"
            "v_lshlrev_b32_sdwa v163, v124, sext(v141) dst_sel:DWORD dst_unused:UNUSED_PAD src0_sel:DWORD src1_sel:BYTE_0\n"
            "ds_add_rtn_u32 v159, v159, v124\n"
            "ds_add_rtn_u32 v160, v160, v124\n"
            "ds_add_rtn_u32 v161, v161, v124\n"
            "ds_add_rtn_u32 v162, v162, v124\n"
            "ds_add_rtn_u32 v163, v163, v124\n"
            "s_waitcnt lgkmcnt(0)\n"
            "ds_write_b16_d16_hi v144, v129 offset:160\n"
            "ds_write_b16 v145, v130 offset:160\n"
            "ds_write_b16_d16_hi v146, v130 offset:160\n"
            "ds_write_b16 v147, v131 offset:160\n"
            "ds_write_b16_d16_hi v148, v131 offset:160\n"
            "ds_write_b16_d16_hi v149, v133 offset:160\n"
            "ds_write_b16 v150, v134 offset:160\n"
            "ds_write_b16_d16_hi v151, v134 offset:160\n"
            "ds_write_b16 v152, v135 offset:160\n"
            "ds_write_b16_d16_hi v153, v135 offset:160\n"
            "ds_write_b16_d16_hi v154, v137 offset:160\n"
            "ds_write_b16 v155, v138 offset:160\n"
            "ds_write_b16_d16_hi v156, v138 offset:160\n"
            "ds_write_b16 v157, v139 offset:160\n"
            "ds_write_b16_d16_hi v158, v139 offset:160\n"
            "ds_write_b16_d16_hi v159, v141 offset:160\n"
            "ds_write_b16 v160, v142 offset:160\n"
            "ds_write_b16_d16_hi v161, v142 offset:160\n"
            "ds_write_b16 v162, v143 offset:160\n"
            "ds_write_b16_d16_hi v163, v143 offset:160\n"
            "s_mov_b64 exec, s[62:63]\n"
            "s_cbranch_execz 3f\n"
            "s_waitcnt vmcnt(0)\n"
            "v_subrev_u32 v184, 16, v185\n"
            "v_cmp_gt_u32 vcc, 0xfffff0, v184\n"
            "v_lshrrev_b32 v184, 24, v185\n"
            "s_nop 3\n"
            "s_cbranch_vccz 15f\n"
            "s_mov_b32 %[ovf], 1\n"
            "s_branch 3f\n"
            "15:\n"
            "v_cmp_eq_u32 vcc, %[me1], v184\n"
            "v_bfe_u32 v184, v185, 14, 10\n"
            "s_nop 3\n"
            "s_mov_b64 exec, vcc\n"
            "ds_add_rtn_u32 v184, v184, v124\n"
            "s_waitcnt lgkmcnt(0)\n"
            "ds_write_b16 v184, v185 offset:160\n"
            "3:\n"
            "s_mov_b64 exec, -1\n"
            "s_waitcnt vmcnt(0) lgkmcnt(0)\n"
            : "=&{v[64:79]}"(g0), "=&{v[80:95]}"(g1), "=&{v[96:111]}"(g2), "=&{v[112:127]}"(g3), "=&{v[60:63]}"(mk), "=&{v[58:59]}"(nv), [ovf] "=&s"(ovf),
              "+{v[160:175]}"(ed), "+{v[176:183]}"(ee)
            : [srw] "s"(srw), [ao] "s"(OFF_ALL + 128 * (bid % NREP)), [so] "s"(OFF_SLOT + me * SLOTB), [co] "v"((i & 7) * 16u),
              [po] "v"(po), [m3] "s"(hw == 3 ? 0xffffffffffull : ~0ull), [mb] "s"(hw < 2 ? ~0ull : hw == 2 ? 0xffffffull : 0ull),
              [ia] "v"(L_IMG + hid * 20),
              [o0] "v"(OFF_SLOT + (hid >> 2) * SLOTB + me * 64 + (hid & 3) * 16), [bo] "s"(OFF_BOX + me * 3 * BOXB), [oo] "s"(OFF_OVF + me * OVFB),
              [fe] "s"(OFF_FE + 64 * me), [i4] "v"(i4), [ob] "s"(OFF_OVF + hw * 16 * OVFB), [me1] "s"(me + 1)
            : "scc", "vcc", "memory", "s80", "s81", "v128", "v129", "v130", "v131", "v132", "v133", "v134", "v135", "v136", "v137", "v138", "v139", "v140", "v141", "v142", "v143", "v144", "v145", "v146", "v147", "v148", "v149", "v150", "v151", "v152", "v153", "v154", "v155", "v156", "v157", "v158", "v159", "v184", "v185", "v186", "v187", "v188", "v189", "v190", "v191", "v192", "v193", "v194", "v195", "v196", "v197", "v198", "v199", "v200", "v201", "v202", "v203", "v204", "v205", "v206", "v207", "v208", "v209", "v210", "v211", "v212", "v213", "v214", "v215", "v216", "v217", "v218", "v219", "v220", "v221", "v222", "v223", "v224", "v225", "v226", "v227", "v228", "v229", "v230", "v231", "v232", "v233", "v234", "v235", "v236", "v237", "v238", "v239", "v240", "v241", "v242", "v243", "s60", "s61", "s62", "s63", "s64", "s65");
        if (ovf) {
#pragma unroll 1
            for (int k = 0; k < 4; ++k) {
                const unsigned w3 = k == 0 ? mk[0] : k == 1 ? mk[1] : k == 2 ? mk[2] : mk[3];
#pragma unroll 1
                for (unsigned long long m = __ballot((w3 & 0xff00u) != 0xff00u); m; m &= m - 1) {
                    const unsigned ob = OFF_OVF + ((k * 256 + hw * 64 + __builtin_ctzll(m)) >> 2) * OVFB;
                    const unsigned n = __builtin_amdgcn_readfirstlane(ld32(rs, ob));
#pragma unroll 1
                    for (unsigned t = lane; t < n; t += 64) {
                        const unsigned en = ld32(rs, ob + 4 + 4 * t), nd = (en >> 16) & 63u;
                        const unsigned end = L_LIST - CBIAS + nd * LROW + 256;
                        if ((en >> 24) == (unsigned)me + 1u && *(volatile LAS unsigned*)((LAS char*)lds + 4 * nd) <= end) {
                            const unsigned r = atomicAdd((unsigned*)(lds + 4 * nd), 2u);
                            if (r < end) *(u16*)(lds + r + CBIAS) = (u16)en;
                        }
                    }
                }
            }
        }
        asm volatile(
            "s_waitcnt lgkmcnt(0)\n"
            "s_mov_b64 exec, 1\n"
            "ds_add_u32 v126, v125 offset:4\n"
            "s_mov_b64 exec, -1\n"
            "5:\n"
            "ds_read_b32 v208, v126 offset:4\n"
            "s_waitcnt lgkmcnt(0)\n"
            "v_cmp_le_u32 vcc, 12, v208\n"
            "s_nop 4\n"
            "s_cbranch_vccnz 6f\n"
            "s_sleep 4\n"
            "s_branch 5b\n"
            "6:\n"
            "s_mov_b64 exec, 1\n"
            "buffer_store_dwordx2 %[nv], off, %[srw], %[pf] sc1\n"
            "s_mov_b64 exec, -1\n"
            "ds_read_b32 v208, %[ca]\n"
            "ds_read_u16 v64, %[la] offset:0\n"
            "ds_read_u16 v66, %[la] offset:8\n"
            "ds_read_u16 v68, %[la] offset:16\n"
            "ds_read_u16 v70, %[la] offset:24\n"
            "ds_read_u16 v72, %[la] offset:528\n"
            "ds_read_u16 v74, %[la] offset:536\n"
            "ds_read_u16 v76, %[la] offset:544\n"
            "ds_read_u16 v78, %[la] offset:552\n"
            "ds_read_u16 v80, %[la] offset:1056\n"
            "ds_read_u16 v82, %[la] offset:1064\n"
            "ds_read_u16 v84, %[la] offset:1072\n"
            "ds_read_u16 v86, %[la] offset:1080\n"
            "ds_read_u16 v88, %[la] offset:1584\n"
            "ds_read_u16 v90, %[la] offset:1592\n"
            "ds_read_u16 v92, %[la] offset:1600\n"
            "ds_read_u16 v94, %[la] offset:1608\n"
            "ds_read_u16 v96, %[la] offset:2112\n"
            "ds_read_u16 v98, %[la] offset:2120\n"
            "ds_read_u16 v100, %[la] offset:2128\n"
            "ds_read_u16 v102, %[la] offset:2136\n"
            "v_add_u32 %[la], 32, %[la]\n"
            "s_waitcnt lgkmcnt(0)\n"
            "v_sub_u32 v208, v208, %[beg]\n"
            "s_mov_b32 s60, 2\n"
            "v_cmp_lt_i32 vcc, 256, v208\n"
            "v_max_i32_dpp v208, v208, v208 quad_perm:[1,0,3,2] row_mask:0xf bank_mask:0xf\n"
            "s_nop 1\n"
            "s_or_b32 %[big], vcc_lo, vcc_hi\n"
            "s_cbranch_scc1 9f\n"
            "v_readlane_b32 s61, v208, 48\n"
            "v_readlane_b32 s62, v208, 50\n"
            "v_readlane_b32 s63, v208, 52\n"
            "v_readlane_b32 s64, v208, 54\n"
            "v_readlane_b32 s65, v208, 56\n"
            "s_setprio 0\n"
            "buffer_load_dwordx4 v[128:131], v[64:65], %[sru], 0 idxen offen sc1\n"
            "s_sleep 2\n"
            "buffer_load_dwordx4 v[132:135], v[66:67], %[sru], 0 idxen offen sc1\n"
            "s_sleep 1\n"
            "buffer_load_dwordx4 v[136:139], v[68:69], %[sru], 0 idxen offen sc1\n"
            "s_sleep 2\n"
            "buffer_load_dwordx4 v[140:143], v[70:71], %[sru], 0 idxen offen sc1\n"
            "s_sleep 1\n"
            "ds_read_u16 v64, %[la] offset:0\n"
            "ds_read_u16 v66, %[la] offset:8\n"
            "ds_read_u16 v68, %[la] offset:16\n"
            "ds_read_u16 v70, %[la] offset:24\n"
            "buffer_load_dwordx4 v[144:147], v[72:73], %[sru], 0 idxen offen sc1\n"
            "s_sleep 2\n"
            "buffer_load_dwordx4 v[148:151], v[74:75], %[sru], 0 idxen offen sc1\n"
            "s_sleep 1\n"
            "buffer_load_dwordx4 v[152:155], v[76:77], %[sru], 0 idxen offen sc1\n"
            "s_sleep 2\n"
            "buffer_load_dwordx4 v[156:159], v[78:79], %[sru], 0 idxen offen sc1\n"
            "s_sleep 1\n"
            "ds_read_u16 v72, %[la] offset:528\n"
            "ds_read_u16 v74, %[la] offset:536\n"
            "ds_read_u16 v76, %[la] offset:544\n"
            "ds_read_u16 v78, %[la] offset:552\n"
            "buffer_load_dwordx4 v[160:163], v[80:81], %[sru], 0 idxen offen sc1\n"
            "s_sleep 2\n"
            "buffer_load_dwordx4 v[164:167], v[82:83], %[sru], 0 idxen offen sc1\n"
            "s_sleep 1\n"
            "buffer_load_dwordx4 v[168:171], v[84:85], %[sru], 0 idxen offen sc1\n"
            "s_sleep 2\n"
            "buffer_load_dwordx4 v[172:175], v[86:87], %[sru], 0 idxen offen sc1\n"
            "s_sleep 1\n"
            "ds_read_u16 v80, %[la] offset:1056\n"
            "ds_read_u16 v82, %[la] offset:1064\n"
            "ds_read_u16 v84, %[la] offset:1072\n"
            "ds_read_u16 v86, %[la] offset:1080\n"
            "buffer_load_dwordx4 v[176:179], v[88:89], %[sru], 0 idxen offen sc1\n"
            "s_sleep 2\n"
            "buffer_load_dwordx4 v[180:183], v[90:91], %[sru], 0 idxen offen sc1\n"
            "s_sleep 1\n"
            "buffer_load_dwordx4 v[184:187], v[92:93], %[sru], 0 idxen offen sc1\n"
            "s_sleep 2\n"
            "buffer_load_dwordx4 v[188:191], v[94:95], %[sru], 0 idxen offen sc1\n"
            "s_sleep 1\n"
            "ds_read_u16 v88, %[la] offset:1584\n"
            "ds_read_u16 v90, %[la] offset:1592\n"
            "ds_read_u16 v92, %[la] offset:1600\n"
            "ds_read_u16 v94, %[la] offset:1608\n"
            "buffer_load_dwordx4 v[192:195], v[96:97], %[sru], 0 idxen offen sc1\n"
            "s_sleep 2\n"
            "buffer_load_dwordx4 v[196:199], v[98:99], %[sru], 0 idxen offen sc1\n"
            "s_sleep 1\n"
            "buffer_load_dwordx4 v[200:203], v[100:101], %[sru], 0 idxen offen sc1\n"
            "s_sleep 2\n"
            "buffer_load_dwordx4 v[204:207], v[102:103], %[sru], 0 idxen offen sc1\n"
            "s_sleep 1\n"
            "ds_read_u16 v96, %[la] offset:2112\n"
            "ds_read_u16 v98, %[la] offset:2120\n"
            "ds_read_u16 v100, %[la] offset:2128\n"
            "ds_read_u16 v102, %[la] offset:2136\n"
            "7:\n"
            "v_add_u32 %[la], 32, %[la]\n"
            "s_waitcnt vmcnt(16) lgkmcnt(12)\n"
            "v_smfmac_f32_16x16x64_bf16 v[104:107], %[sel], v[128:135], %[sidx]\n"
            "buffer_load_dwordx4 v[128:131], v[64:65], %[sru], 0 idxen offen sc1\n"
            "s_sleep 2\n"
            "buffer_load_dwordx4 v[132:135], v[66:67], %[sru], 0 idxen offen sc1\n"
            "s_sleep 1\n"
            "v_smfmac_f32_16x16x64_bf16 v[104:107], %[sel], v[136:143], %[sidx]\n"
            "buffer_load_dwordx4 v[136:139], v[68:69], %[sru], 0 idxen offen sc1\n"
            "s_sleep 2\n"
            "buffer_load_dwordx4 v[140:143], v[70:71], %[sru], 0 idxen offen sc1\n"
            "s_sleep 1\n"
            "ds_read_u16 v64, %[la] offset:0\n"
            "ds_read_u16 v66, %[la] offset:8\n"
            "ds_read_u16 v68, %[la] offset:16\n"
            "ds_read_u16 v70, %[la] offset:24\n"
            "s_waitcnt vmcnt(16) lgkmcnt(12)\n"
            "v_smfmac_f32_16x16x64_bf16 v[108:111], %[sel], v[144:151], %[sidx]\n"
            "buffer_load_dwordx4 v[144:147], v[72:73], %[sru], 0 idxen offen sc1\n"
            "s_sleep 2\n"
            "buffer_load_dwordx4 v[148:151], v[74:75], %[sru], 0 idxen offen sc1\n"
            "s_sleep 1\n"
            "v_smfmac_f32_16x16x64_bf16 v[108:111], %[sel], v[152:159], %[sidx]\n"
            "buffer_load_dwordx4 v[152:155], v[76:77], %[sru], 0 idxen offen sc1\n"
            "s_sleep 2\n"
            "buffer_load_dwordx4 v[156:159], v[78:79], %[sru], 0 idxen offen sc1\n"
            "s_sleep 1\n"
            "ds_read_u16 v72, %[la] offset:528\n"
            "ds_read_u16 v74, %[la] offset:536\n"
            "ds_read_u16 v76, %[la] offset:544\n"
            "ds_read_u16 v78, %[la] offset:552\n"
            "s_waitcnt vmcnt(16) lgkmcnt(12)\n"
            "v_smfmac_f32_16x16x64_bf16 v[112:115], %[sel], v[160:167], %[sidx]\n"
            "buffer_load_dwordx4 v[160:163], v[80:81], %[sru], 0 idxen offen sc1\n"
            "s_sleep 2\n"
            "buffer_load_dwordx4 v[164:167], v[82:83], %[sru], 0 idxen offen sc1\n"
            "s_sleep 1\n"
            "v_smfmac_f32_16x16x64_bf16 v[112:115], %[sel], v[168:175], %[sidx]\n"
            "buffer_load_dwordx4 v[168:171], v[84:85], %[sru], 0 idxen offen sc1\n"
            "s_sleep 2\n"
            "buffer_load_dwordx4 v[172:175], v[86:87], %[sru], 0 idxen offen sc1\n"
            "s_sleep 1\n"
            "ds_read_u16 v80, %[la] offset:1056\n"
            "ds_read_u16 v82, %[la] offset:1064\n"
            "ds_read_u16 v84, %[la] offset:1072\n"
            "ds_read_u16 v86, %[la] offset:1080\n"
            "s_waitcnt vmcnt(16) lgkmcnt(12)\n"
            "v_smfmac_f32_16x16x64_bf16 v[116:119], %[sel], v[176:183], %[sidx]\n"
            "buffer_load_dwordx4 v[176:179], v[88:89], %[sru], 0 idxen offen sc1\n"
            "s_sleep 2\n"
            "buffer_load_dwordx4 v[180:183], v[90:91], %[sru], 0 idxen offen sc1\n"
            "s_sleep 1\n"
            "v_smfmac_f32_16x16x64_bf16 v[116:119], %[sel], v[184:191], %[sidx]\n"
            "buffer_load_dwordx4 v[184:187], v[92:93], %[sru], 0 idxen offen sc1\n"
            "s_sleep 2\n"
            "buffer_load_dwordx4 v[188:191], v[94:95], %[sru], 0 idxen offen sc1\n"
            "s_sleep 1\n"
            "ds_read_u16 v88, %[la] offset:1584\n"
            "ds_read_u16 v90, %[la] offset:1592\n"
            "ds_read_u16 v92, %[la] offset:1600\n"
            "ds_read_u16 v94, %[la] offset:1608\n"
            "s_waitcnt vmcnt(16) lgkmcnt(12)\n"
            "v_smfmac_f32_16x16x64_bf16 v[120:123], %[sel], v[192:199], %[sidx]\n"
            "buffer_load_dwordx4 v[192:195], v[96:97], %[sru], 0 idxen offen sc1\n"
            "s_sleep 2\n"
            "buffer_load_dwordx4 v[196:199], v[98:99], %[sru], 0 idxen offen sc1\n"
            "s_sleep 1\n"
            "v_smfmac_f32_16x16x64_bf16 v[120:123], %[sel], v[200:207], %[sidx]\n"
            "buffer_load_dwordx4 v[200:203], v[100:101], %[sru], 0 idxen offen sc1\n"
            "s_sleep 2\n"
            "buffer_load_dwordx4 v[204:207], v[102:103], %[sru], 0 idxen offen sc1\n"
            "s_sleep 1\n"
            "ds_read_u16 v96, %[la] offset:2112\n"
            "ds_read_u16 v98, %[la] offset:2120\n"
            "ds_read_u16 v100, %[la] offset:2128\n"
            "ds_read_u16 v102, %[la] offset:2136\n"
            "s_sub_u32 s60, s60, 1\n"
            "s_cbranch_scc0 7b\n"
            "s_sub_i32 s61, s61, 96\n"
            "s_sub_i32 s62, s62, 96\n"
            "s_sub_i32 s63, s63, 96\n"
            "s_sub_i32 s64, s64, 96\n"
            "s_sub_i32 s65, s65, 96\n"
            "8:\n"
            "v_add_u32 %[la], 32, %[la]\n"
            "s_mov_b32 s60, 0\n"
            "s_waitcnt vmcnt(0) lgkmcnt(0)\n"
            "s_cmp_le_i32 s61, 0\n"
            "s_cbranch_scc1 20f\n"
            "v_smfmac_f32_16x16x64_bf16 v[104:107], %[sel], v[128:135], %[sidx]\n"
            "s_cmp_le_i32 s61, 32\n"
            "s_cbranch_scc1 100f\n"
            "s_mov_b32 s60, 1\n"
            "buffer_load_dwordx4 v[128:131], v[64:65], %[sru], 0 idxen offen sc1\n"
            "s_sleep 1\n"
            "buffer_load_dwordx4 v[132:135], v[66:67], %[sru], 0 idxen offen sc1\n"
            "s_sleep 1\n"
            "ds_read_u16 v64, %[la] offset:0\n"
            "ds_read_u16 v66, %[la] offset:8\n"
            "100:\n"
            "s_cmp_le_i32 s61, 16\n"
            "s_cbranch_scc1 20f\n"
            "v_smfmac_f32_16x16x64_bf16 v[104:107], %[sel], v[136:143], %[sidx]\n"
            "s_cmp_le_i32 s61, 48\n"
            "s_cbranch_scc1 101f\n"
            "s_mov_b32 s60, 1\n"
            "buffer_load_dwordx4 v[136:139], v[68:69], %[sru], 0 idxen offen sc1\n"
            "s_sleep 1\n"
            "buffer_load_dwordx4 v[140:143], v[70:71], %[sru], 0 idxen offen sc1\n"
            "s_sleep 1\n"
            "ds_read_u16 v68, %[la] offset:16\n"
            "ds_read_u16 v70, %[la] offset:24\n"
            "101:\n"
            "20:\n"
            "s_sub_i32 s61, s61, 32\n"
            "s_cmp_le_i32 s62, 0\n"
            "s_cbranch_scc1 21f\n"
            "v_smfmac_f32_16x16x64_bf16 v[108:111], %[sel], v[144:151], %[sidx]\n"
            "s_cmp_le_i32 s62, 32\n"
            "s_cbranch_scc1 110f\n"
            "s_mov_b32 s60, 1\n"
            "buffer_load_dwordx4 v[144:147], v[72:73], %[sru], 0 idxen offen sc1\n"
            "s_sleep 1\n"
            "buffer_load_dwordx4 v[148:151], v[74:75], %[sru], 0 idxen offen sc1\n"
            "s_sleep 1\n"
            "ds_read_u16 v72, %[la] offset:528\n"
            "ds_read_u16 v74, %[la] offset:536\n"
            "110:\n"
            "s_cmp_le_i32 s62, 16\n"
            "s_cbranch_scc1 21f\n"
            "v_smfmac_f32_16x16x64_bf16 v[108:111], %[sel], v[152:159], %[sidx]\n"
            "s_cmp_le_i32 s62, 48\n"
            "s_cbranch_scc1 111f\n"
            "s_mov_b32 s60, 1\n"
            "buffer_load_dwordx4 v[152:155], v[76:77], %[sru], 0 idxen offen sc1\n"
            "s_sleep 1\n"
            "buffer_load_dwordx4 v[156:159], v[78:79], %[sru], 0 idxen offen sc1\n"
            "s_sleep 1\n"
            "ds_read_u16 v76, %[la] offset:544\n"
            "ds_read_u16 v78, %[la] offset:552\n"
            "111:\n"
            "21:\n"
            "s_sub_i32 s62, s62, 32\n"
            "s_cmp_le_i32 s63, 0\n"
            "s_cbranch_scc1 22f\n"
            "v_smfmac_f32_16x16x64_bf16 v[112:115], %[sel], v[160:167], %[sidx]\n"
            "s_cmp_le_i32 s63, 32\n"
            "s_cbranch_scc1 120f\n"
            "s_mov_b32 s60, 1\n"
            "buffer_load_dwordx4 v[160:163], v[80:81], %[sru], 0 idxen offen sc1\n"
            "s_sleep 1\n"
            "buffer_load_dwordx4 v[164:167], v[82:83], %[sru], 0 idxen offen sc1\n"
            "s_sleep 1\n"
            "ds_read_u16 v80, %[la] offset:1056\n"
            "ds_read_u16 v82, %[la] offset:1064\n"
            "120:\n"
            "s_cmp_le_i32 s63, 16\n"
            "s_cbranch_scc1 22f\n"
            "v_smfmac_f32_16x16x64_bf16 v[112:115], %[sel], v[168:175], %[sidx]\n"
            "s_cmp_le_i32 s63, 48\n"
            "s_cbranch_scc1 121f\n"
            "s_mov_b32 s60, 1\n"
            "buffer_load_dwordx4 v[168:171], v[84:85], %[sru], 0 idxen offen sc1\n"
            "s_sleep 1\n"
            "buffer_load_dwordx4 v[172:175], v[86:87], %[sru], 0 idxen offen sc1\n"
            "s_sleep 1\n"
            "ds_read_u16 v84, %[la] offset:1072\n"
            "ds_read_u16 v86, %[la] offset:1080\n"
            "121:\n"
            "22:\n"
            "s_sub_i32 s63, s63, 32\n"
            "s_cmp_le_i32 s64, 0\n"
            "s_cbranch_scc1 23f\n"
            "v_smfmac_f32_16x16x64_bf16 v[116:119], %[sel], v[176:183], %[sidx]\n"
            "s_cmp_le_i32 s64, 32\n"
            "s_cbranch_scc1 130f\n"
            "s_mov_b32 s60, 1\n"
            "buffer_load_dwordx4 v[176:179], v[88:89], %[sru], 0 idxen offen sc1\n"
            "s_sleep 1\n"
            "buffer_load_dwordx4 v[180:183], v[90:91], %[sru], 0 idxen offen sc1\n"
            "s_sleep 1\n"
            "ds_read_u16 v88, %[la] offset:1584\n"
            "ds_read_u16 v90, %[la] offset:1592\n"
            "130:\n"
            "s_cmp_le_i32 s64, 16\n"
            "s_cbranch_scc1 23f\n"
            "v_smfmac_f32_16x16x64_bf16 v[116:119], %[sel], v[184:191], %[sidx]\n"
            "s_cmp_le_i32 s64, 48\n"
            "s_cbranch_scc1 131f\n"
            "s_mov_b32 s60, 1\n"
            "buffer_load_dwordx4 v[184:187], v[92:93], %[sru], 0 idxen offen sc1\n"
            "s_sleep 1\n"
            "buffer_load_dwordx4 v[188:191], v[94:95], %[sru], 0 idxen offen sc1\n"
            "s_sleep 1\n"
            "ds_read_u16 v92, %[la] offset:1600\n"
            "ds_read_u16 v94, %[la] offset:1608\n"
            "131:\n"
            "23:\n"
            "s_sub_i32 s64, s64, 32\n"
            "s_cmp_le_i32 s65, 0\n"
            "s_cbranch_scc1 24f\n"
            "v_smfmac_f32_16x16x64_bf16 v[120:123], %[sel], v[192:199], %[sidx]\n"
            "s_cmp_le_i32 s65, 32\n"
            "s_cbranch_scc1 140f\n"
            "s_mov_b32 s60, 1\n"
            "buffer_load_dwordx4 v[192:195], v[96:97], %[sru], 0 idxen offen sc1\n"
            "s_sleep 1\n"
            "buffer_load_dwordx4 v[196:199], v[98:99], %[sru], 0 idxen offen sc1\n"
            "s_sleep 1\n"
            "ds_read_u16 v96, %[la] offset:2112\n"
            "ds_read_u16 v98, %[la] offset:2120\n"
            "140:\n"
            "s_cmp_le_i32 s65, 16\n"
            "s_cbranch_scc1 24f\n"
            "v_smfmac_f32_16x16x64_bf16 v[120:123], %[sel], v[200:207], %[sidx]\n"
            "s_cmp_le_i32 s65, 48\n"
            "s_cbranch_scc1 141f\n"
            "s_mov_b32 s60, 1\n"
            "buffer_load_dwordx4 v[200:203], v[100:101], %[sru], 0 idxen offen sc1\n"
            "s_sleep 1\n"
            "buffer_load_dwordx4 v[204:207], v[102:103], %[sru], 0 idxen offen sc1\n"
            "s_sleep 1\n"
            "ds_read_u16 v100, %[la] offset:2128\n"
            "ds_read_u16 v102, %[la] offset:2136\n"
            "141:\n"
            "24:\n"
            "s_sub_i32 s65, s65, 32\n"
            "s_cmp_lg_u32 s60, 0\n"
            "s_cbranch_scc1 8b\n"
            "s_mov_b32 exec_hi, 0\n"
            "s_nop 7\n"
            "ds_write_b128 %[za], v[104:107] offset:0\n"
            "ds_write_b128 %[za], v[108:111] offset:544\n"
            "ds_write_b128 %[za], v[112:115] offset:1088\n"
            "ds_write_b128 %[za], v[116:119] offset:1632\n"
            "ds_write_b128 %[za], v[120:123] offset:2176\n"
            "s_mov_b64 exec, -1\n"
            "9:\n"
            "s_waitcnt lgkmcnt(0)\n"
            "s_setprio 0\n"
            : "+{v[64:79]}"(g0), "+{v[80:95]}"(g1), "+{v[96:111]}"(g2), "+{v[112:127]}"(g3), [la] "+v"(la), [big] "=&s"(big)
            : [sru] "s"(sru), [srw] "s"(srw), [nv] "v"(nv), [pf] "s"(hw == 0 ? OFF_PF + 64 * me : WS_END), [sel] "v"(sel), [sidx] "v"(sidx), [za] "v"(za), [ca] "v"(ca), [beg] "v"(beg)
            : "scc", "vcc", "memory", "v128", "v129", "v130", "v131", "v132", "v133", "v134", "v135", "v136", "v137", "v138", "v139", "v140", "v141", "v142", "v143", "v144", "v145", "v146", "v147", "v148", "v149", "v150", "v151", "v152", "v153", "v154", "v155", "v156", "v157", "v158", "v159", "v160", "v161", "v162", "v163", "v164", "v165", "v166", "v167", "v168", "v169", "v170", "v171", "v172", "v173", "v174", "v175", "v176", "v177", "v178", "v179", "v180", "v181", "v182", "v183", "v184", "v185", "v186", "v187", "v188", "v189", "v190", "v191", "v192", "v193", "v194", "v195", "v196", "v197", "v198", "v199", "v200", "v201", "v202", "v203", "v204", "v205", "v206", "v207", "v208", "s60", "s61", "s62", "s63", "s64", "s65");
        if (big) {
#pragma unroll 1
            for (int k = hid; k < (int)(NPB * ZROW / 4); k += 256) *(unsigned*)(lds + L_ZL + 4 * k) = 0;
            rbar<8>(lds, L_ACT, 8, lane);
#pragma unroll 1
            for (int e0 = hid; e0 < NE; e0 += 256) {
                const unsigned nd = dst[e0] - me * NPB, sv = src[e0];
#pragma unroll 1
                for (unsigned long long m = __ballot(nd < (unsigned)NPB); m; m &= m - 1) {
                    const int l = __builtin_ctzll(m);
                    const unsigned v = __builtin_amdgcn_raw_buffer_load_b16(rs, OFF_U + __builtin_amdgcn_readlane(sv, l) * 128 + 2 * lane, 0, 16);
                    atomicAdd((float*)(lds + L_ZL + __builtin_amdgcn_readlane(nd, l) * ZROW + 4 * lane), bflo(v));
                }
            }
        }
    }
    const size_t ob = (size_t)out;
    const u32x4 sro = {(unsigned)ob, (unsigned)(ob >> 32) & 0xffffu, NN * 128u, 0x20000u};
    const unsigned sb = OFF_BOX + ((me ^ 1) * 3 + 1) * BOXB;
    asm volatile(
            "s_setprio 0\n"
            "v_add_u32 v222, 49152, %[pa]\n"
            "v_lshrrev_b32 v228, 1, %[l16]\n"
            "v_mov_b32 v209, 0\n"
            "s_cmp_eq_u32 %[hw], 3\n"
            "s_cbranch_scc1 1f\n"
            "v_lshrrev_b32 v220, 7, %[xo]\n"
            "v_lshlrev_b32 v220, 1, v220\n"
            "s_movk_i32 s60, 24576\n"
            "buffer_load_dwordx4 v[128:131], %[l16], %[srw], s60 offen sc1\n"
            "s_sleep 1\n"
            "s_movk_i32 s60, 25600\n"
            "buffer_load_dwordx4 v[132:135], %[l16], %[srw], s60 offen sc1\n"
            "s_sleep 1\n"
            "s_movk_i32 s60, 26624\n"
            "buffer_load_dwordx4 v[136:139], %[l16], %[srw], s60 offen sc1\n"
            "s_sleep 1\n"
            "s_movk_i32 s60, 27648\n"
            "buffer_load_dwordx4 v[140:143], %[l16], %[srw], s60 offen sc1\n"
            "s_sleep 1\n"
            "global_load_dwordx4 v[144:147], %[xo], %[x]\n"
            "s_sleep 1\n"
            "global_load_dwordx4 v[148:151], %[xo], %[x] offset:64\n"
            "s_sleep 1\n"
            "global_load_ushort v220, v220, %[di]\n"
            "ds_read_b128 v[212:215], %[qa]\n"
            "ds_read_b128 v[216:219], %[qa] offset:1024\n"
            "s_movk_i32 s60, 16384\n"
            "buffer_load_dwordx4 v[96:99], %[l16], %[srw], s60 offen sc1\n"
            "s_sleep 1\n"
            "s_movk_i32 s60, 17408\n"
            "buffer_load_dwordx4 v[100:103], %[l16], %[srw], s60 offen sc1\n"
            "s_sleep 1\n"
            "s_movk_i32 s60, 18432\n"
            "buffer_load_dwordx4 v[104:107], %[l16], %[srw], s60 offen sc1\n"
            "s_sleep 1\n"
            "s_movk_i32 s60, 19456\n"
            "buffer_load_dwordx4 v[108:111], %[l16], %[srw], s60 offen sc1\n"
            "s_sleep 1\n"
            "s_movk_i32 s60, 20480\n"
            "buffer_load_dwordx4 v[112:115], %[l16], %[srw], s60 offen sc1\n"
            "s_sleep 1\n"
            "s_movk_i32 s60, 21504\n"
            "buffer_load_dwordx4 v[116:119], %[l16], %[srw], s60 offen sc1\n"
            "s_sleep 1\n"
            "s_movk_i32 s60, 22528\n"
            "buffer_load_dwordx4 v[120:123], %[l16], %[srw], s60 offen sc1\n"
            "s_sleep 1\n"
            "s_movk_i32 s60, 23552\n"
            "buffer_load_dwordx4 v[124:127], %[l16], %[srw], s60 offen sc1\n"
            "s_sleep 1\n"
            "s_movk_i32 s60, 8192\n"
            "buffer_load_dwordx4 v[64:67], %[l16], %[srw], s60 offen sc1\n"
            "s_sleep 1\n"
            "s_movk_i32 s60, 9216\n"
            "buffer_load_dwordx4 v[68:71], %[l16], %[srw], s60 offen sc1\n"
            "s_sleep 1\n"
            "s_movk_i32 s60, 10240\n"
            "buffer_load_dwordx4 v[72:75], %[l16], %[srw], s60 offen sc1\n"
            "s_sleep 1\n"
            "s_movk_i32 s60, 11264\n"
            "buffer_load_dwordx4 v[76:79], %[l16], %[srw], s60 offen sc1\n"
            "s_sleep 1\n"
            "s_movk_i32 s60, 12288\n"
            "buffer_load_dwordx4 v[80:83], %[l16], %[srw], s60 offen sc1\n"
            "s_sleep 1\n"
            "s_movk_i32 s60, 13312\n"
            "buffer_load_dwordx4 v[84:87], %[l16], %[srw], s60 offen sc1\n"
            "s_sleep 1\n"
            "s_movk_i32 s60, 14336\n"
            "buffer_load_dwordx4 v[88:91], %[l16], %[srw], s60 offen sc1\n"
            "s_sleep 1\n"
            "s_movk_i32 s60, 15360\n"
            "buffer_load_dwordx4 v[92:95], %[l16], %[srw], s60 offen sc1\n"
            "s_sleep 1\n"
            "s_waitcnt vmcnt(16) lgkmcnt(0)\n"
            "v_mfma_f32_16x16x32_bf16 v[224:227], v[128:131], v[212:215], 0\n"
            "v_lshlrev_b32 v152, 16, v144\n"
            "v_and_b32 v153, 0xffff0000, v144\n"
            "v_mfma_f32_16x16x32_bf16 v[224:227], v[132:135], v[216:219], v[224:227]\n"
            "v_lshlrev_b32 v154, 16, v145\n"
            "v_and_b32 v155, 0xffff0000, v145\n"
            "v_lshlrev_b32 v156, 16, v146\n"
            "v_and_b32 v157, 0xffff0000, v146\n"
            "v_lshlrev_b32 v158, 16, v147\n"
            "v_and_b32 v159, 0xffff0000, v147\n"
            "v_lshlrev_b32 v160, 16, v148\n"
            "v_and_b32 v161, 0xffff0000, v148\n"
            "v_exp_f32 v224, v224\n"
            "v_exp_f32 v225, v225\n"
            "v_exp_f32 v226, v226\n"
            "v_exp_f32 v227, v227\n"
            "v_cvt_pk_bf16_f32 v212, v224, v225\n"
            "v_cvt_pk_bf16_f32 v213, v226, v227\n"
            "v_lshlrev_b32 v162, 16, v149\n"
            "v_and_b32 v163, 0xffff0000, v149\n"
            "v_mfma_f32_16x16x16_bf16 v[168:171], v[136:137], v[212:213], 0\n"
            "v_lshlrev_b32 v164, 16, v150\n"
            "v_and_b32 v165, 0xffff0000, v150\n"
            "v_mfma_f32_16x16x16_bf16 v[172:175], v[138:139], v[212:213], 0\n"
            "v_lshlrev_b32 v166, 16, v151\n"
            "v_and_b32 v167, 0xffff0000, v151\n"
            "v_mfma_f32_16x16x16_bf16 v[176:179], v[140:141], v[212:213], 0\n"
            "v_lshlrev_b32 v220, 16, v220\n"
            "v_add_u32 v223, 49152, %[pb]\n"
            "v_mfma_f32_16x16x16_bf16 v[180:183], v[142:143], v[212:213], 0\n"
            "s_nop 7\n"
            "1:\n"
            "s_setprio 2\n"
            "s_mov_b32 s60, 1048576\n"
            "2:\n"
            "buffer_load_dwordx2 v[200:201], off, %[srw], %[pf] sc1\n"
            "s_waitcnt vmcnt(0)\n"
            "v_cmp_ne_u64 vcc, %[nv], v[200:201]\n"
            "s_sub_u32 s60, s60, 1\n"
            "s_cbranch_scc1 3f\n"
            "s_nop 2\n"
            "s_cbranch_vccnz 2b\n"
            "3:\n"
            "s_sub_u32 s60, %[sb], %[sd]\n"
            "s_add_u32 s61, %[sb], %[sd]\n"
            "s_cmp_eq_u32 %[hw], 3\n"
            "s_waitcnt lgkmcnt(0)\n"
            "s_barrier\n"
            "ds_read_b128 v[184:187], %[pa] offset:16384\n"
            "ds_read_b128 v[188:191], %[pa] offset:40960\n"
            "ds_read_b128 v[192:195], v222 offset:16384\n"
            "ds_read_b128 v[196:199], v222 offset:40960\n"
            "ds_read_b128 v[128:131], %[pa] offset:20480\n"
            "ds_read_b128 v[132:135], %[pa] offset:45056\n"
            "ds_read_b128 v[136:139], v222 offset:20480\n"
            "ds_read_b128 v[140:143], v222 offset:45056\n"
            "s_waitcnt lgkmcnt(4)\n"
            "v_pk_add_f32 v[184:185], v[184:185], v[188:189]\n"
            "v_pk_add_f32 v[186:187], v[186:187], v[190:191]\n"
            "v_pk_add_f32 v[192:193], v[192:193], v[196:197]\n"
            "v_pk_add_f32 v[194:195], v[194:195], v[198:199]\n"
            "v_pk_add_f32 v[184:185], v[184:185], v[192:193]\n"
            "v_pk_add_f32 v[186:187], v[186:187], v[194:195]\n"
            "v_cvt_pk_bf16_f32 v188, v184, v185\n"
            "v_cvt_pk_bf16_f32 v189, v186, v187\n"
            "v_min_u32 v188, -2, v188\n"
            "v_min_u32 v189, -2, v189\n"
            "buffer_store_dwordx2 v[188:189], v228, %[srw], s60 offen sc1\n"
            "s_cbranch_scc0 4f\n"
            "v_min_u32 v187, -2, v187\n"
            "s_mov_b32 exec_lo, 0\n"
            "s_mov_b32 exec_hi, 0xffff0000\n"
            "buffer_store_dword v187, %[i4], %[srw], s60 offen offset:512 sc1\n"
            "s_mov_b64 exec, -1\n"
            "4:\n"
            "s_waitcnt lgkmcnt(0)\n"
            "v_pk_add_f32 v[128:129], v[128:129], v[132:133]\n"
            "v_pk_add_f32 v[130:131], v[130:131], v[134:135]\n"
            "v_pk_add_f32 v[136:137], v[136:137], v[140:141]\n"
            "v_pk_add_f32 v[138:139], v[138:139], v[142:143]\n"
            "v_pk_add_f32 v[128:129], v[128:129], v[136:137]\n"
            "v_pk_add_f32 v[130:131], v[130:131], v[138:139]\n"
            "v_cvt_pk_bf16_f32 v132, v128, v129\n"
            "v_cvt_pk_bf16_f32 v133, v130, v131\n"
            "v_min_u32 v132, -2, v132\n"
            "v_min_u32 v133, -2, v133\n"
            "buffer_store_dwordx2 v[132:133], v228, %[srw], %[sb] offen sc1\n"
            "s_cbranch_scc0 5f\n"
            "v_min_u32 v131, -2, v131\n"
            "s_mov_b32 exec_lo, 0\n"
            "s_mov_b32 exec_hi, 0xffff0000\n"
            "buffer_store_dword v131, %[i4], %[srw], %[sb] offen offset:512 sc1\n"
            "s_mov_b64 exec, -1\n"
            "5:\n"
            "s_barrier\n"
            "ds_read_b128 v[184:187], %[pa] offset:0\n"
            "ds_read_b128 v[188:191], %[pa] offset:24576\n"
            "ds_read_b128 v[192:195], v222 offset:0\n"
            "ds_read_b128 v[196:199], v222 offset:24576\n"
            "ds_read_b128 v[128:131], %[zo] offset:0\n"
            "ds_read_b128 v[132:135], %[zo] offset:64\n"
            "ds_read_b128 v[136:139], %[zo] offset:128\n"
            "ds_read_b128 v[140:143], %[zo] offset:192\n"
            "ds_read_b32 v224, v209 offset:248\n"
            "s_waitcnt lgkmcnt(5)\n"
            "v_pk_add_f32 v[184:185], v[184:185], v[188:189]\n"
            "v_pk_add_f32 v[186:187], v[186:187], v[190:191]\n"
            "v_pk_add_f32 v[192:193], v[192:193], v[196:197]\n"
            "v_pk_add_f32 v[194:195], v[194:195], v[198:199]\n"
            "v_pk_add_f32 v[184:185], v[184:185], v[192:193]\n"
            "v_pk_add_f32 v[186:187], v[186:187], v[194:195]\n"
            "v_cvt_pk_bf16_f32 v188, v184, v185\n"
            "v_cvt_pk_bf16_f32 v189, v186, v187\n"
            "v_min_u32 v188, -2, v188\n"
            "v_min_u32 v189, -2, v189\n"
            "buffer_store_dwordx2 v[188:189], v228, %[srw], s61 offen sc1\n"
            "s_cbranch_scc0 6f\n"
            "v_min_u32 v187, -2, v187\n"
            "s_mov_b32 exec_lo, 0\n"
            "s_mov_b32 exec_hi, 0xffff0000\n"
            "buffer_store_dword v187, %[i4], %[srw], s61 offen offset:512 sc1\n"
            "s_mov_b64 exec, -1\n"
            "6:\n"
            "s_cbranch_scc0 7f\n"
            "s_waitcnt lgkmcnt(0)\n"
            "s_barrier\n"
            "s_branch 99f\n"
            "7:\n"
            "s_setprio 0\n"
            "s_waitcnt lgkmcnt(0)\n"
            "v_cvt_pk_bf16_f32 v144, v128, v129\n"
            "v_cvt_pk_bf16_f32 v145, v130, v131\n"
            "v_cvt_pk_bf16_f32 v146, v132, v133\n"
            "v_cvt_pk_bf16_f32 v147, v134, v135\n"
            "v_cvt_pk_bf16_f32 v148, v136, v137\n"
            "v_cvt_pk_bf16_f32 v149, v138, v139\n"
            "v_cvt_pk_bf16_f32 v150, v140, v141\n"
            "v_cvt_pk_bf16_f32 v151, v142, v143\n"
            "v_mfma_f32_16x16x32_bf16 v[128:131], v[96:99], v[144:147], 0\n"
            "v_cmp_ne_u32 vcc, 0, v224\n"
            "v_mfma_f32_16x16x32_bf16 v[132:135], v[104:107], v[144:147], 0\n"
            "v_mfma_f32_16x16x32_bf16 v[136:139], v[112:115], v[144:147], 0\n"
            "v_mfma_f32_16x16x32_bf16 v[140:143], v[120:123], v[144:147], 0\n"
            "v_mfma_f32_16x16x32_bf16 v[128:131], v[100:103], v[148:151], v[128:131]\n"
            "v_mfma_f32_16x16x32_bf16 v[132:135], v[108:111], v[148:151], v[132:135]\n"
            "v_mfma_f32_16x16x32_bf16 v[136:139], v[116:119], v[148:151], v[136:139]\n"
            "v_mfma_f32_16x16x32_bf16 v[140:143], v[124:127], v[148:151], v[140:143]\n"
            "s_nop 4\n"
            "v_pk_fma_f32 v[152:153], v[128:129], v[220:221], v[152:153] op_sel_hi:[1,0,1]\n"
            "v_pk_fma_f32 v[154:155], v[130:131], v[220:221], v[154:155] op_sel_hi:[1,0,1]\n"
            "v_pk_fma_f32 v[156:157], v[132:133], v[220:221], v[156:157] op_sel_hi:[1,0,1]\n"
            "v_pk_fma_f32 v[158:159], v[134:135], v[220:221], v[158:159] op_sel_hi:[1,0,1]\n"
            "v_pk_fma_f32 v[160:161], v[136:137], v[220:221], v[160:161] op_sel_hi:[1,0,1]\n"
            "v_pk_fma_f32 v[162:163], v[138:139], v[220:221], v[162:163] op_sel_hi:[1,0,1]\n"
            "v_pk_fma_f32 v[164:165], v[140:141], v[220:221], v[164:165] op_sel_hi:[1,0,1]\n"
            "v_pk_fma_f32 v[166:167], v[142:143], v[220:221], v[166:167] op_sel_hi:[1,0,1]\n"
            "s_nop 1\n"
            "s_nop 2\n"
            "s_cbranch_vccz 8f\n"
            "v_mov_b64 v[168:169], 0\n"
            "v_mov_b64 v[170:171], 0\n"
            "v_mov_b64 v[172:173], 0\n"
            "v_mov_b64 v[174:175], 0\n"
            "v_mov_b64 v[176:177], 0\n"
            "v_mov_b64 v[178:179], 0\n"
            "v_mov_b64 v[180:181], 0\n"
            "v_mov_b64 v[182:183], 0\n"
            "8:\n"
            "s_mov_b32 s60, 2097152\n"
            "buffer_load_dwordx2 v[200:201], v228, %[srw], %[ib] offen offset:0 sc1\n"
            "buffer_load_dwordx2 v[202:203], v228, %[srw], %[ib] offen offset:512 sc1\n"
            "buffer_load_dwordx2 v[204:205], v228, %[srw], %[ib] offen offset:1024 sc1\n"
            "buffer_load_dwordx2 v[206:207], v228, %[srw], %[ib] offen offset:1536 sc1\n"
            "buffer_load_dword v208, %[i4], %[srw], %[ib] offen offset:2048 sc1\n"
            "s_setprio 3\n"
            "s_barrier\n"
            "ds_read_b128 v[232:235], %[pb] offset:3072\n"
            "ds_read_b128 v[236:239], %[pb] offset:27648\n"
            "ds_read_b128 v[240:243], v223 offset:3072\n"
            "ds_read_b128 v[244:247], v223 offset:27648\n"
            "ds_read_b128 v[128:131], %[pb] offset:2048\n"
            "ds_read_b128 v[132:135], %[pb] offset:26624\n"
            "ds_read_b128 v[136:139], v223 offset:2048\n"
            "ds_read_b128 v[140:143], v223 offset:26624\n"
            "ds_read_b128 v[96:99], %[pb] offset:0\n"
            "ds_read_b128 v[100:103], %[pb] offset:24576\n"
            "ds_read_b128 v[104:107], v223 offset:0\n"
            "ds_read_b128 v[108:111], v223 offset:24576\n"
            "ds_read_b128 v[112:115], %[pb] offset:1024\n"
            "ds_read_b128 v[116:119], %[pb] offset:25600\n"
            "ds_read_b128 v[120:123], v223 offset:1024\n"
            "ds_read_b128 v[124:127], v223 offset:25600\n"
            "s_waitcnt lgkmcnt(12)\n"
            "v_pk_add_f32 v[232:233], v[232:233], v[236:237]\n"
            "v_pk_add_f32 v[234:235], v[234:235], v[238:239]\n"
            "v_pk_add_f32 v[240:241], v[240:241], v[244:245]\n"
            "v_pk_add_f32 v[242:243], v[242:243], v[246:247]\n"
            "v_pk_add_f32 v[180:181], v[180:181], v[232:233]\n"
            "v_pk_add_f32 v[182:183], v[182:183], v[234:235]\n"
            "v_pk_add_f32 v[180:181], v[180:181], v[240:241]\n"
            "v_pk_add_f32 v[182:183], v[182:183], v[242:243]\n"
            "s_waitcnt lgkmcnt(8)\n"
            "v_pk_add_f32 v[128:129], v[128:129], v[132:133]\n"
            "v_pk_add_f32 v[130:131], v[130:131], v[134:135]\n"
            "v_mfma_f32_16x16x4_f32 v[224:227], %[g3], v183, 0\n"
            "v_pk_add_f32 v[136:137], v[136:137], v[140:141]\n"
            "v_pk_add_f32 v[138:139], v[138:139], v[142:143]\n"
            "v_pk_add_f32 v[176:177], v[176:177], v[128:129]\n"
            "v_pk_add_f32 v[178:179], v[178:179], v[130:131]\n"
            "v_pk_add_f32 v[176:177], v[176:177], v[136:137]\n"
            "v_pk_add_f32 v[178:179], v[178:179], v[138:139]\n"
            "v_cvt_pk_bf16_f32 v148, v176, v177\n"
            "v_cvt_pk_bf16_f32 v149, v178, v179\n"
            "v_cvt_pk_bf16_f32 v150, v180, v181\n"
            "v_cvt_pk_bf16_f32 v151, v182, v183\n"
            "s_waitcnt lgkmcnt(4)\n"
            "v_pk_add_f32 v[96:97], v[96:97], v[100:101]\n"
            "v_pk_add_f32 v[98:99], v[98:99], v[102:103]\n"
            "v_mfma_f32_16x16x32_bf16 v[184:187], v[68:71], v[148:151], 0\n"
            "v_pk_add_f32 v[104:105], v[104:105], v[108:109]\n"
            "v_pk_add_f32 v[106:107], v[106:107], v[110:111]\n"
            "v_mfma_f32_16x16x32_bf16 v[188:191], v[76:79], v[148:151], 0\n"
            "v_pk_add_f32 v[168:169], v[168:169], v[96:97]\n"
            "v_pk_add_f32 v[170:171], v[170:171], v[98:99]\n"
            "v_mfma_f32_16x16x32_bf16 v[192:195], v[84:87], v[148:151], 0\n"
            "v_pk_add_f32 v[168:169], v[168:169], v[104:105]\n"
            "v_pk_add_f32 v[170:171], v[170:171], v[106:107]\n"
            "v_mfma_f32_16x16x32_bf16 v[196:199], v[92:95], v[148:151], 0\n"
            "s_waitcnt lgkmcnt(0)\n"
            "v_pk_add_f32 v[112:113], v[112:113], v[116:117]\n"
            "v_pk_add_f32 v[114:115], v[114:115], v[118:119]\n"
            "v_pk_add_f32 v[120:121], v[120:121], v[124:125]\n"
            "v_pk_add_f32 v[122:123], v[122:123], v[126:127]\n"
            "v_pk_add_f32 v[172:173], v[172:173], v[112:113]\n"
            "v_pk_add_f32 v[174:175], v[174:175], v[114:115]\n"
            "v_pk_add_f32 v[172:173], v[172:173], v[120:121]\n"
            "v_pk_add_f32 v[174:175], v[174:175], v[122:123]\n"
            "v_cvt_pk_bf16_f32 v144, v168, v169\n"
            "v_cvt_pk_bf16_f32 v145, v170, v171\n"
            "v_cvt_pk_bf16_f32 v146, v172, v173\n"
            "v_cvt_pk_bf16_f32 v147, v174, v175\n"
            "s_nop 1\n"
            "v_mfma_f32_16x16x32_bf16 v[184:187], v[64:67], v[144:147], v[184:187]\n"
            "v_mfma_f32_16x16x32_bf16 v[188:191], v[72:75], v[144:147], v[188:191]\n"
            "v_mfma_f32_16x16x32_bf16 v[192:195], v[80:83], v[144:147], v[192:195]\n"
            "v_mfma_f32_16x16x32_bf16 v[196:199], v[88:91], v[144:147], v[196:199]\n"
            "s_branch 11f\n"
            "9:\n"
            "buffer_load_dwordx2 v[200:201], v228, %[srw], %[ib] offen offset:0 sc1\n"
            "buffer_load_dwordx2 v[202:203], v228, %[srw], %[ib] offen offset:512 sc1\n"
            "buffer_load_dwordx2 v[204:205], v228, %[srw], %[ib] offen offset:1024 sc1\n"
            "buffer_load_dwordx2 v[206:207], v228, %[srw], %[ib] offen offset:1536 sc1\n"
            "buffer_load_dword v208, %[i4], %[srw], %[ib] offen offset:2048 sc1\n"
            "11:\n"
            "s_waitcnt vmcnt(0)\n"
            "v_max3_u32 v229, v200, v201, v202\n"
            "v_max3_u32 v229, v229, v203, v204\n"
            "v_max3_u32 v229, v229, v205, v206\n"
            "v_max3_u32 v229, v229, v207, v208\n"
            "v_cmp_eq_u32 vcc, -1, v229\n"
            "v_add_f32 v220, v224, v208\n"
            "s_sub_u32 s60, s60, 1\n"
            "v_rcp_f32 v220, v220\n"
            "s_cbranch_scc1 10f\n"
            "s_nop 0\n"
            "s_cbranch_vccnz 9b\n"
            "10:\n"
            "v_mfma_f32_16x16x32_bf16 v[184:187], v[64:67], v[200:203], v[184:187]\n"
            "v_mfma_f32_16x16x32_bf16 v[184:187], v[68:71], v[204:207], v[184:187]\n"
            "v_mfma_f32_16x16x32_bf16 v[188:191], v[72:75], v[200:203], v[188:191]\n"
            "v_mfma_f32_16x16x32_bf16 v[188:191], v[76:79], v[204:207], v[188:191]\n"
            "v_mfma_f32_16x16x32_bf16 v[192:195], v[80:83], v[200:203], v[192:195]\n"
            "v_mfma_f32_16x16x32_bf16 v[192:195], v[84:87], v[204:207], v[192:195]\n"
            "v_mfma_f32_16x16x32_bf16 v[196:199], v[88:91], v[200:203], v[196:199]\n"
            "s_nop 2\n"
            "v_pk_fma_f32 v[152:153], v[184:185], v[220:221], v[152:153] op_sel_hi:[1,0,1]\n"
            "v_mfma_f32_16x16x32_bf16 v[196:199], v[92:95], v[204:207], v[196:199]\n"
            "v_pk_fma_f32 v[154:155], v[186:187], v[220:221], v[154:155] op_sel_hi:[1,0,1]\n"
            "v_cvt_pk_bf16_f32 v144, v152, v153\n"
            "v_cvt_pk_bf16_f32 v145, v154, v155\n"
            "v_pk_fma_f32 v[156:157], v[188:189], v[220:221], v[156:157] op_sel_hi:[1,0,1]\n"
            "v_pk_fma_f32 v[158:159], v[190:191], v[220:221], v[158:159] op_sel_hi:[1,0,1]\n"
            "v_cvt_pk_bf16_f32 v146, v156, v157\n"
            "v_cvt_pk_bf16_f32 v147, v158, v159\n"
            "s_mov_b64 exec, %[vm]\n"
            "buffer_store_dwordx4 v[144:147], %[xo], %[sro], 0 offen sc1\n"
            "v_pk_fma_f32 v[160:161], v[192:193], v[220:221], v[160:161] op_sel_hi:[1,0,1]\n"
            "v_pk_fma_f32 v[162:163], v[194:195], v[220:221], v[162:163] op_sel_hi:[1,0,1]\n"
            "v_cvt_pk_bf16_f32 v148, v160, v161\n"
            "v_cvt_pk_bf16_f32 v149, v162, v163\n"
            "v_pk_fma_f32 v[164:165], v[196:197], v[220:221], v[164:165] op_sel_hi:[1,0,1]\n"
            "v_pk_fma_f32 v[166:167], v[198:199], v[220:221], v[166:167] op_sel_hi:[1,0,1]\n"
            "v_cvt_pk_bf16_f32 v150, v164, v165\n"
            "v_cvt_pk_bf16_f32 v151, v166, v167\n"
            "buffer_store_dwordx4 v[148:151], %[xo], %[sro], 0 offen offset:64 sc1\n"
            "s_mov_b64 exec, -1\n"
            "99:\n"
        :
        : [l16] "v"(lane16), [xo] "v"(xo), [zo] "v"(zo), [pa] "v"(pa), [pb] "v"(po2), [qa] "v"(qa), [i4] "v"(i4), [g3] "v"(g3), [nv] "v"(nv),
          [srw] "s"(srw), [sro] "s"(sro), [x] "s"(x), [di] "s"(dinv), [hw] "s"(hw), [pf] "s"(OFF_PF + 64 * (me ^ 1)), [sb] "s"(sb + hw * 512),
          [sd] "s"(half ? BOXB : -BOXB), [ib] "s"(OFF_BOX + (me * 3 + hw) * BOXB), [vm] "s"(slot != 2 ? ~0ull : half ? 0xff00ff00ff00ff00ull : 0x00ff00ff00ff00ffull)
        : "scc", "vcc", "memory", "v64", "v65", "v66", "v67", "v68", "v69", "v70", "v71", "v72", "v73", "v74", "v75", "v76", "v77", "v78", "v79", "v80", "v81", "v82", "v83", "v84", "v85", "v86", "v87", "v88", "v89", "v90", "v91", "v92", "v93", "v94", "v95", "v96", "v97", "v98", "v99", "v100", "v101", "v102", "v103", "v104", "v105", "v106", "v107", "v108", "v109", "v110", "v111", "v112", "v113", "v114", "v115", "v116", "v117", "v118", "v119", "v120", "v121", "v122", "v123", "v124", "v125", "v126", "v127", "v128", "v129", "v130", "v131", "v132", "v133", "v134", "v135", "v136", "v137", "v138", "v139", "v140", "v141", "v142", "v143", "v144", "v145", "v146", "v147", "v148", "v149", "v150", "v151", "v152", "v153", "v154", "v155", "v156", "v157", "v158", "v159", "v160", "v161", "v162", "v163", "v164", "v165", "v166", "v167", "v168", "v169", "v170", "v171", "v172", "v173", "v174", "v175", "v176", "v177", "v178", "v179", "v180", "v181", "v182", "v183", "v184", "v185", "v186", "v187", "v188", "v189", "v190", "v191", "v192", "v193", "v194", "v195", "v196", "v197", "v198", "v199", "v200", "v201", "v202", "v203", "v204", "v205", "v206", "v207", "v208", "v209", "v210", "v211", "v212", "v213", "v214", "v215", "v216", "v217", "v218", "v219", "v220", "v221", "v222", "v223", "v224", "v225", "v226", "v227", "v228", "v229", "v230", "v231", "v232", "v233", "v234", "v235", "v236", "v237", "v238", "v239", "v240", "v241", "v242", "v243", "v244", "v245", "v246", "v247", "s60", "s61");
}

extern "C" void kernel_launch(void* const* d_in, const int* in_sizes, int n_in, void* d_out, int out_size, void* d_ws, size_t ws_size,
                              hipStream_t stream) {
    k_gps<<<256, 512, 0, stream>>>((char*)d_ws, (const u16*)d_in[0], (const u16*)d_in[5], (const u16*)d_in[6], (const int*)d_in[2], (const int*)d_in[1],
                                   (const u16*)d_in[3], (u16*)d_out, (const u16*)d_in[4], (const u16*)d_in[7], (const u16*)d_in[8]);
}
```
